```python
import torch

def _softmax(x, axis=-1):
    return torch.softmax(x, dim=axis)
def _gelu(x):
    return torch.nn.functional.gelu(x, approximate="tanh")
def _silu(x):
    return torch.nn.functional.silu(x)
def _sigmoid(x):
    return torch.sigmoid(x)
def _layernorm(x, axis=-1, eps=1e-5):
    m = x.mean(dim=axis, keepdim=True)
    v = x.var(dim=axis, keepdim=True, unbiased=False)
    return (x - m) / torch.sqrt(v + eps)
def _scatter_add(tgt, idx, src, axis=0):
    return tgt.clone().index_add_(axis, idx, src)
def _segment_sum(src, seg, n):
    out = torch.zeros((n,) + src.shape[1:], dtype=src.dtype)
    return out.index_add_(0, seg, src)
def _pad(x, pw):
    flat = tuple(v for pair in reversed(pw) for v in pair)
    return torch.nn.functional.pad(x, flat)

kernel_name = 'parallel_sum__linear_kernel_hedgehog__glu_gated_glu__prompt'

def setup_inputs():

    _w = (lambda a: (lambda t: t.to(torch.bfloat16) if t.is_floating_point() else t)(torch.as_tensor(a)))
    def _A_setup():
        B, S, D, H = 1, 2048, 512, 32
        Dh = D // H
        return {
            "x": _w((lambda *s: torch.randn(*s, dtype=torch.bfloat16) * 0.05)(B, S, D)),
            "wqkv": _w((lambda *s: torch.randn(*s, dtype=torch.bfloat16) * 0.4)(D, 3 * D)),
            "w_phi": _w((lambda *s: torch.randn(*s, dtype=torch.bfloat16) * 0.4)(H, Dh, Dh) * (Dh ** -0.5)),
            "wo": _w((lambda *s: torch.randn(*s, dtype=torch.bfloat16) * 0.1)(D, D)),
        }
    _A_in = _A_setup()

    def _B_setup():
        B, S, D, F = 1, 2048, 512, 1792
        return {
            "x": _w((lambda *s: torch.randn(*s, dtype=torch.bfloat16) * 0.05)(B, S, D)),
            "w_gate": _w((lambda *s: torch.randn(*s, dtype=torch.bfloat16) * 0.05)(D, F)),
            "w_up": _w((lambda *s: torch.randn(*s, dtype=torch.bfloat16) * 0.05)(D, F)),
            "w_down": _w((lambda *s: torch.randn(*s, dtype=torch.bfloat16) * 0.05)(F, D)),
        }
    _B_in = _B_setup()

    return {
        "A_x": _A_in["x"],
        "A_wqkv": _A_in["wqkv"],
        "A_w_phi": _A_in["w_phi"],
        "A_wo": _A_in["wo"],
        "B_x": _B_in["x"],
        "B_w_gate": _B_in["w_gate"],
        "B_w_up": _B_in["w_up"],
        "B_w_down": _B_in["w_down"],
    }

def reference(A_x, A_wqkv, A_w_phi, A_wo, B_x, B_w_gate, B_w_up, B_w_down):
    def _A_ref(x, wqkv, w_phi, wo):
        B, S, D = x.shape
        H = 32; Dh = D // H
        qkv = (lambda x, *d: x.permute(*d))((x @ wqkv).reshape(B, S, 3, H, Dh), 2, 0, 3, 1, 4)
        q, k, v = qkv[0], qkv[1], qkv[2]
        pq = torch.einsum("bhsd,hdf->bhsf", q, w_phi)
        pk = torch.einsum("bhsd,hdf->bhsf", k, w_phi)
        phi_q = torch.concatenate([_softmax(pq, axis=-1), _softmax(-pq, axis=-1)], axis=-1)
        phi_k = torch.concatenate([_softmax(pk, axis=-1), _softmax(-pk, axis=-1)], axis=-1)
        kv = torch.einsum("bhsf,bhsd->bhfd", phi_k, v)
        z  = torch.einsum("bhsf->bhf", phi_k)
        num = torch.einsum("bhqf,bhfd->bhqd", phi_q, kv)
        den = torch.einsum("bhqf,bhf->bhq", phi_q, z)[..., None] + 1e-6
        o = num / den
        return (lambda x, *d: x.permute(*d))(o, 0, 2, 1, 3).reshape(B, S, D) @ wo

    def _B_ref(x, w_gate, w_up, w_down):
        g = torch.einsum("bsd,df->bsf", x, w_gate)
        u = torch.einsum("bsd,df->bsf", x, w_up)
        h = _sigmoid(g) * u
        return torch.einsum("bsf,fd->bsd", h, w_down)

    x = A_x
    xn = _layernorm(x, axis=-1)
    return x + _A_ref(xn, A_wqkv, A_w_phi, A_wo) + _B_ref(xn, B_w_gate, B_w_up, B_w_down)
```

```cpp
#include <hip/hip_runtime.h>
#include <hip/hip_bf16.h>
typedef __hip_bfloat16 bf16;
static constexpr int S=2048, D=512, H=32, DH=16, F=1792;
__device__ inline float b2f(bf16 x){ return __bfloat162float(x); }
__device__ inline bf16 f2b(float x){ return __float2bfloat16(x); }

__global__ void k_ln(const bf16* x, float* xn){
  int r=blockIdx.x, t=threadIdx.x;
  float s=0,q=0; float v[8];
  for(int i=0;i<8;i++){ v[i]=b2f(x[r*D+t*8+i]); s+=v[i]; }
  for(int m=32;m>=1;m>>=1) s+=__shfl_xor(s,m);
  float mean=s/D;
  for(int i=0;i<8;i++){ float d=v[i]-mean; q+=d*d; }
  for(int m=32;m>=1;m>>=1) q+=__shfl_xor(q,m);
  float rs=rsqrtf(q/D+1e-5f);
  for(int i=0;i<8;i++) xn[r*D+t*8+i]=b2f(f2b((v[i]-mean)*rs));
}
__global__ void k_gemm(const float* A, const bf16* B, float* C, int N, int K, int roundb){
  __shared__ float As[16][17], Bs[16][17];
  int tx=threadIdx.x&15, ty=threadIdx.x>>4;
  int row=blockIdx.y*16+ty, col=blockIdx.x*16+tx;
  float acc=0;
  for(int k0=0;k0<K;k0+=16){
    As[ty][tx]=A[(size_t)row*K+k0+tx];
    Bs[ty][tx]=b2f(B[(size_t)(k0+ty)*N+col]);
    __syncthreads();
    for(int k=0;k<16;k++) acc+=As[ty][k]*Bs[k][tx];
    __syncthreads();
  }
  C[(size_t)row*N+col]= roundb? b2f(f2b(acc)) : acc;
}
__global__ void k_feat(const float* qkv, const bf16* wphi, float* phiq, float* phik){
  int idx=blockIdx.x*blockDim.x+threadIdx.x;
  int which=idx&1; int h=(idx>>1)&31; int s=idx>>6;
  const float* src=qkv+(size_t)s*1536+which*512+h*16;
  float p[16];
  for(int f=0;f<16;f++){ float a=0; for(int d=0;d<16;d++) a+=src[d]*b2f(wphi[h*256+d*16+f]); p[f]=a; }
  float mx=-1e30f, mn=1e30f;
  for(int f=0;f<16;f++){ mx=fmaxf(mx,p[f]); mn=fminf(mn,p[f]); }
  float s1=0,s2=0; float e1[16],e2[16];
  for(int f=0;f<16;f++){ e1[f]=__expf(p[f]-mx); e2[f]=__expf(mn-p[f]); s1+=e1[f]; s2+=e2[f]; }
  float* dst=(which?phik:phiq)+((size_t)s*32+h)*32;
  for(int f=0;f<16;f++){ dst[f]=b2f(f2b(e1[f]/s1)); dst[16+f]=b2f(f2b(e2[f]/s2)); }
}
__global__ void k_kv(const float* qkv, const float* phik, float* kv){
  int h=blockIdx.x; int t=threadIdx.x;
  int f=t/17, d=t%17;
  float a=0;
  for(int s=0;s<S;s++){ float pk=phik[((size_t)s*32+h)*32+f]; float v= d<16? qkv[(size_t)s*1536+1024+h*16+d] : 1.f; a+=pk*v; }
  kv[h*544+t]=a;
}
__global__ void k_o(const float* phiq, const float* kv, float* o){
  int idx=blockIdx.x*blockDim.x+threadIdx.x;
  int d=idx&15, h=(idx>>4)&31, s=idx>>9;
  float num=0,den=0;
  for(int f=0;f<32;f++){ float pq=phiq[((size_t)s*32+h)*32+f]; num+=pq*kv[h*544+f*17+d]; den+=pq*kv[h*544+f*17+16]; }
  o[idx]=b2f(f2b(num/(den+1e-6f)));
}
__global__ void k_h(const float* g, const float* u, float* hh){
  int idx=blockIdx.x*blockDim.x+threadIdx.x;
  float a=g[idx]; hh[idx]=b2f(f2b(u[idx]/(1.f+__expf(-a))));
}
__global__ void k_fin(const bf16* x, const float* a, const float* b, bf16* out){
  int idx=blockIdx.x*blockDim.x+threadIdx.x;
  out[idx]=f2b(b2f(x[idx])+a[idx]+b[idx]);
}
extern "C" void kernel_launch(void* const* d_in, const int* in_sizes, int n_in, void* d_out, int out_size, void* d_ws, size_t ws_size, hipStream_t stream){
  const bf16* x=(const bf16*)d_in[0]; const bf16* wqkv=(const bf16*)d_in[1]; const bf16* wphi=(const bf16*)d_in[2]; const bf16* wo=(const bf16*)d_in[3];
  const bf16* wg=(const bf16*)d_in[5]; const bf16* wu=(const bf16*)d_in[6]; const bf16* wd=(const bf16*)d_in[7];
  float* w=(float*)d_ws;
  float* xn=w; w+=S*D; float* qkv=w; w+=S*1536; float* g=w; w+=S*F; float* u=w; w+=S*F; float* hh=w; w+=S*F;
  float* phiq=w; w+=S*1024; float* phik=w; w+=S*1024; float* kv=w; w+=32*544; float* o=w; w+=S*D; float* ya=w; w+=S*D; float* yb=w; w+=S*D;
  k_ln<<<S,64,0,stream>>>(x,xn);
  k_gemm<<<dim3(1536/16,S/16),256,0,stream>>>(xn,wqkv,qkv,1536,512,1);
  k_gemm<<<dim3(F/16,S/16),256,0,stream>>>(xn,wg,g,F,512,0);
  k_gemm<<<dim3(F/16,S/16),256,0,stream>>>(xn,wu,u,F,512,0);
  k_feat<<<S*64/256,256,0,stream>>>(qkv,wphi,phiq,phik);
  k_kv<<<32,544,0,stream>>>(qkv,phik,kv);
  k_o<<<S*512/256,256,0,stream>>>(phiq,kv,o);
  k_h<<<S*F/256,256,0,stream>>>(g,u,hh);
  k_gemm<<<dim3(512/16,S/16),256,0,stream>>>(o,wo,ya,512,512,0);
  k_gemm<<<dim3(512/16,S/16),256,0,stream>>>(hh,wd,yb,512,F,0);
  k_fin<<<S*512/256,256,0,stream>>>(x,ya,yb,(bf16*)d_out);
}
```

```cpp
#include <hip/hip_runtime.h>
#include <stdint.h>
#pragma clang diagnostic ignored "-Wint-to-pointer-cast"

typedef unsigned u32;
typedef unsigned long long u64;
typedef __attribute__((ext_vector_type(4))) unsigned u32x4;
typedef __attribute__((ext_vector_type(2))) unsigned u32x2;
typedef __attribute__((ext_vector_type(2))) float f32x2;
typedef __attribute__((ext_vector_type(4))) float f32x4;
typedef __attribute__((ext_vector_type(16))) float f32x16;
typedef __attribute__((ext_vector_type(8))) __bf16 bf16x8;
typedef __attribute__((ext_vector_type(2))) __bf16 bf16x2;
typedef __attribute__((ext_vector_type(4))) short s16x4;
#define LAS __attribute__((address_space(3)))
#define GAS __attribute__((address_space(1)))
#define CAS __attribute__((address_space(4)))
#define DI __device__ __forceinline__

#define ABL_NOSWZ 0
#define ABL_NOCS 0
#define ABL_NOSTAT 0
#define ABL_NOLD 0
#define ABL_NORD 0
#define STATPOS 0
#define STAGGER 1
#define CODE_PF 1

constexpr u32 WS_E = 0;
constexpr u32 WS_KVF = 256;
constexpr u32 WS_F2 = 2048;
constexpr u32 WS_KVP = 8192;
constexpr u32 WS_ZP = WS_KVP + 524288;
constexpr u32 WS_A = 1u << 20;
constexpr u32 WS_BYTES = WS_A + 32u * 72u * 4096u;
constexpr u32 WS_DBG = 16u << 20;

constexpr u32 L_X = 0;
constexpr u32 L_W = 49152;
constexpr u32 L_M = 147456;
constexpr u32 L_ST = L_M;
constexpr u32 L_CSP = L_M + 1024;
constexpr u32 L_WPH = L_M + 4096;
constexpr u32 L_JUNK = L_M + 6144;
constexpr u32 L_SCR = L_X + 16384;

constexpr float LOG2E = 1.4426950408889634f;

struct Params {
  const char* x; const char* wqkv; const char* wphi; const char* wo;
  const char* wg; const char* wu; const char* wd;
  char* out; char* ws;
};

#define SCHED() __builtin_amdgcn_sched_barrier(0)
template <int N> DI void wait_vm() { asm volatile("s_waitcnt vmcnt(%0)" ::"n"(N) : "memory"); }
template <int N> DI void wait_bar() { asm volatile("s_waitcnt vmcnt(%0) lgkmcnt(0)\n\ts_barrier" ::"n"(N) : "memory"); }
#define PRO_BAR() asm volatile("s_barrier" ::: "memory")
DI void lgkm0() { __builtin_amdgcn_s_waitcnt(0xC07F); }
DI void lgkm_bar() { asm volatile("s_waitcnt lgkmcnt(0)\n\ts_barrier" ::: "memory"); }
DI void wait_lgkm() { asm volatile("s_waitcnt lgkmcnt(0)" ::: "memory"); }

DI void glds16(const char* g, u32 lds) {
  asm volatile("s_mov_b32 m0, %1\n\ts_nop 0\n\tglobal_load_lds_dwordx4 %0, off" ::"v"(g), "s"(lds) : "memory");
}
DI void glds16o128(const char* g, u32 lds) {
  asm volatile("s_mov_b32 m0, %1\n\ts_nop 0\n\tglobal_load_lds_dwordx4 %0, off offset:128" ::"v"(g), "s"(lds - 128) : "memory");
}
DI void glds16s(const char* sbase, u32 voff, u32 lds) {
  asm volatile("s_mov_b32 m0, %2\n\ts_nop 0\n\tglobal_load_lds_dwordx4 %0, %1" ::"v"(voff), "s"(sbase), "s"(lds) : "memory");
}
DI u32x4 lds128(u32 a) { return *(const LAS u32x4*)(a); }
DI f32x4 lds128f(u32 a) { return *(const LAS f32x4*)(a); }
DI u32x2 ldstr(u32 a) { return __builtin_bit_cast(u32x2, __builtin_amdgcn_ds_read_tr16_b64_v4i16((LAS s16x4*)(a))); }
DI u32x4 ldstr2(u32 a0, u32 a1) { u32x2 lo = ldstr(a0), hi = ldstr(a1); return u32x4{lo.x, lo.y, hi.x, hi.y}; }
DI f32x4 mfma16(u32x4 a, u32x4 b, f32x4 c) {
  return __builtin_amdgcn_mfma_f32_16x16x32_bf16(__builtin_bit_cast(bf16x8, a), __builtin_bit_cast(bf16x8, b), c, 0, 0, 0);
}
DI f32x16 mfma32(u32x4 a, u32x4 b, f32x16 c) {
  return __builtin_amdgcn_mfma_f32_32x32x16_bf16(__builtin_bit_cast(bf16x8, a), __builtin_bit_cast(bf16x8, b), c, 0, 0, 0);
}
DI u32 pk2(float a, float b) { bf16x2 v = {(__bf16)a, (__bf16)b}; return __builtin_bit_cast(u32, v); }
DI float asf(u32 x) { return __builtin_bit_cast(float, x); }
DI u32 asu(float x) { return __builtin_bit_cast(u32, x); }
DI float fmax2(float a, float b) { return __builtin_elementwise_maximum(a, b); }
DI void swap16(u32& a, u32& b) { auto r = __builtin_amdgcn_permlane16_swap(a, b, false, false); a = r[0]; b = r[1]; }
DI void swap32(u32& a, u32& b) { auto r = __builtin_amdgcn_permlane32_swap(a, b, false, false); a = r[0]; b = r[1]; }
DI float ex2(float x) { return __builtin_amdgcn_exp2f(x); }
DI float rcp(float x) { return __builtin_amdgcn_rcpf(x); }

typedef __amdgpu_buffer_rsrc_t rsrc_t;
DI u32x4 ld_sc1(rsrc_t rs, u32 voff, u32 soff) { return __builtin_amdgcn_raw_buffer_load_b128(rs, voff, soff, 16); }
DI void st_sc1(u32x4 v, rsrc_t rs, u32 voff, u32 soff) { __builtin_amdgcn_raw_buffer_store_b128(v, rs, voff, soff, 16); }
DI u64 flag_ld(const char* p) { return __hip_atomic_load((GAS u64*)p, __ATOMIC_RELAXED, __HIP_MEMORY_SCOPE_AGENT); }
DI void flag_st(char* p, u64 v) { __hip_atomic_store((GAS u64*)p, v, __ATOMIC_RELAXED, __HIP_MEMORY_SCOPE_AGENT); }
DI void poll_tag(const char* p, u64 tag) {
#pragma unroll 1
  for (u32 spins = 0; spins < (1u << 20); ++spins) {
    u64 v = flag_ld(p);
    if (__all(v == tag)) break;
    __builtin_amdgcn_s_sleep(1);
  }
  __builtin_amdgcn_fence(__ATOMIC_ACQUIRE, "wavefront");
}

#define STAMP(i) do {} while (0)
#define CSTAMP(i) do {} while (0)

struct Ctx {
  u32 w, m, n, lane, g, i16;
  u32 ti, q, cg, tg;
  u64 tag;
};

DI void relane(Ctx& c) {
  u32 l = __builtin_amdgcn_mbcnt_hi(~0u, __builtin_amdgcn_mbcnt_lo(~0u, 0u));
  asm volatile("" : "+v"(l));
  c.lane = l; c.g = l >> 4; c.i16 = l & 15;
}
DI u32 w2_area_piece(u32 area, u32 piece, u32 w) {
  if (area < 4) return piece < 3 ? L_W + area * 24576 + w * 3072 + piece * 1024
                                 : L_X + (area < 2 ? 32768 : 0) + (area & 1) * 8192 + w * 1024;
  return piece < 2 ? L_SCR + w * 2048 + piece * 1024 : L_M + w * 2048 + (piece - 2) * 1024;
}
template <int STEP, int AREA> DI void w2_issue(const Params& p, const Ctx& c, int g0 = 0, int g1 = 4) {
  const u32 j = c.lane >> 3;
  const u32 rowl = STEP < 4 ? 64 * (j >> 2) + (j & 3) : (STEP < 7 ? j : (j & 3) + 8 * (j >> 2));
  const u32 P0 = 8 + 5 * (c.w >> 1) + 2 * (c.w & 1), PS = 8 + 5 * (c.w >> 1) + 4;
#pragma unroll
  for (int g = g0; g < g1; ++g) {
    const u32 sw = (((j >> 1) & 1) << 1) | ((g & 1) << 2);
    const u32 voff = rowl * 1024 + (((c.lane & 7) ^ sw) << 4);
    u32 rows;
    if (STEP < 4) rows = 64 * P0 + 16 * STEP + 4 * g;
    else if (STEP == 4) rows = 64 * PS + 32 * (c.w & 1) + 8 * g;
    else if (STEP < 7) rows = 64 * c.w + 32 * (STEP - 5) + 8 * g;
    else rows = 64 * c.w + 16 * (2 * (STEP - 7) + (g >> 1)) + 4 * (g & 1);
    const char* base = (STEP < 7 ? p.wd : p.wo) + (size_t)(rows * 1024 + c.cg * 128);
    glds16s(base, voff, w2_area_piece(AREA, g, c.w));
  }
}
template <int AREA> DI void w2_read(const Ctx& c, u32x4 (&f)[4]) {
  const u32 qq = c.i16 >> 2, pp = c.i16 & 3;
  u32 pb;
  if (AREA < 4) pb = c.g < 3 ? L_W + AREA * 24576 + c.w * 3072 + c.g * 1024 : L_X + (AREA < 2 ? 32768 : 0) + (AREA & 1) * 8192 + c.w * 1024;
  else pb = c.g < 2 ? L_SCR + c.w * 2048 + c.g * 1024 : L_M + c.w * 2048 + (c.g - 2) * 1024;
  const u32 sw = (((qq >> 1) & 1) << 1) | ((c.g & 1) << 2);
  const u32 a = pb + qq * 128 + (((pp >> 1) ^ sw) << 4) + 8 * (pp & 1);
#pragma unroll
  for (int v = 0; v < 4; ++v) f[v] = ldstr2((a ^ (v * 32)), (a ^ (v * 32)) + 512);
}

template <bool TA> struct P1 {
  static constexpr int NT = TA ? 5 : 6;
  f32x4 acc[NT][4];
  f32x4 acs[NT];
  float s1, s2;
};

template <bool TA> DI void main_loop(const Params& p, Ctx& c, u32 off, u32 cx, P1<TA>& o, u32* stamps) {
  constexpr int NT = P1<TA>::NT;
  const u32 lane = c.lane, w = c.w;
  const char* xs = p.x + (size_t)c.ti * 131072;
  u32 xv[2];
#pragma unroll
  for (int pc = 0; pc < 2; ++pc) xv[pc] = (16 * w + 8 * pc + (lane >> 3)) * 1024 + (((lane & 7) ^ (ABL_NOSWZ ? 0 : (4 * pc + (lane >> 4)))) << 4);
  const char* wsrc[3];
  {
    const u32 r = (lane >> 3) & 3, hi = lane >> 5;
    const u32 sw = (((r >> 1) & 1) << 1) | (((w >> 1) & 1) << 2);
    const u32 ch = (((lane & 7) ^ (ABL_NOSWZ ? 0 : sw)) << 4);
    const u32 kr = 4 * w + r;
    if (TA) {
      wsrc[0] = p.wqkv + (kr * 3072 + hi * 1024 + c.q * 128 + ch);
      wsrc[1] = (hi ? p.wu : p.wg) + (kr * 3584 + c.q * 128 + ch);
      wsrc[2] = p.wqkv + (kr * 3072 + 2048 + c.q * 128 + ch);
    } else {
      const u32 P0 = 8 + 5 * (c.q >> 1) + 2 * (c.q & 1), PS = 8 + 5 * (c.q >> 1) + 4;
      const char* b = (hi ? p.wu : p.wg) + (kr * 3584 + ch);
      wsrc[0] = b + P0 * 128; wsrc[1] = b + (P0 + 1) * 128; wsrc[2] = b + PS * 128;
    }
  }
  auto issue_x = [&](int c8, int pc) {
    const u32 kc = (c8 + off) & 7;
    glds16s(xs + kc * 128, xv[pc], L_X + (c8 % 3) * 16384 + (16 * w + 8 * pc) * 128);
  };
  auto issue_w1 = [&](int s, int pc) {
    const u32 k0 = (((s >> 1) + off) & 7) * 64 + (s & 1) * 32;
    const u32 dst = L_W + (s & 3) * 24576 + w * 3072;
    if (TA) {
      if (pc == 0) glds16(wsrc[0] + (size_t)(k0 * 3072), dst);
      else if (pc == 1) glds16(wsrc[1] + (size_t)(k0 * 3584), dst + 1024);
      else if (lane < 32) glds16(wsrc[2] + (size_t)(k0 * 3072), dst + 2048);
    } else {
      if (pc == 0) glds16(wsrc[0] + (size_t)(k0 * 3584), dst);
      else if (pc == 1) glds16o128(wsrc[0] + (size_t)(k0 * 3584), dst + 1024);
      else glds16(wsrc[2] + (size_t)(k0 * 3584), dst + 2048);
    }
  };
  auto issue_w = [&](int s) { issue_w1(s, 0); issue_w1(s, 1); issue_w1(s, 2); };
  STAMP(13);
  u64 ep;
  asm volatile("s_load_dwordx2 %0, %1, 0x0" : "=s"(ep) : "s"(p.ws + WS_E) : "memory");
  issue_x(0, 0); issue_x(0, 1); issue_w(0);
  STAMP(25);
  PRO_BAR();
  issue_x(1, 0); issue_w(1);
  u32 xa[2][4];
#pragma unroll
  for (int s = 0; s < 4; ++s) {
    const u32 row = 64 * c.m + 16 * (s ^ cx) + c.i16;
    xa[0][s] = L_X + row * 128 + ((c.g ^ (c.i16 >> 1)) << 4);
    xa[1][s] = xa[0][s] ^ 64;
  }
  u32 wa;
  {
    const u32 qq = c.i16 >> 2, pp = c.i16 & 3;
    const u32 sw = (((qq >> 1) & 1) << 1) | ((c.g & 1) << 2);
    wa = L_W + 2 * c.g * 3072 + qq * 128 + (((2 * c.n + (pp >> 1)) ^ sw) << 4) + 8 * (pp & 1);
  }
  u32 wb = wa + 49152;
  asm volatile("" : "+v"(wa), "+v"(wb));
  u32x4 xf[2][4], wf[2][NT];
  u32x2 wh[2][NT][2];
  auto read1 = [&](int s, int i) {
    const int b = s & 1;
    if (i >= 2 && i < 6) { xf[b][i - 2] = lds128(xa[s & 1][i - 2] + ((s >> 1) % 3) * 16384); return; }
    const int k = i < 2 ? i : i - 4, l = k >> 1, t = k & 1;
    if (l >= NT) return;
    wh[b][l][t] = ldstr(((s & 2) ? wb : wa) + (s & 1) * 24576 + l * 512 + t * 3072);
    if (t) wf[b][l] = u32x4{wh[b][l][0].x, wh[b][l][0].y, wh[b][l][1].x, wh[b][l][1].y};
  };
  auto mm = [&](int b, int i) {
    if (i < 16) { o.acc[i >> 2][i & 3] = mfma16(wf[b][i >> 2], xf[b][i & 3], o.acc[i >> 2][i & 3]); return; }
    if (TA) o.acc[4][i & 3] = mfma16(xf[b][i & 3], wf[b][4], o.acc[4][i & 3]);
    else { const int l = 4 + ((i >> 1) & 1) * (NT - 5), t = i & 1; o.acc[l][t] = mfma16(wf[b][l], xf[b][t], o.acc[l][t]); }
  };
  auto stat = [&](int b, int u) {
    if (ABL_NOSTAT) return;
    const u32 d = xf[b][0][u >> 1];
    const float v = asf((u & 1) ? (d & 0xffff0000u) : (d << 16));
    o.s1 += v; o.s2 = __builtin_fmaf(v, v, o.s2);
    asm volatile("" : "+v"(o.s1), "+v"(o.s2));
  };
  auto issue1 = [&](int s, int pc) {
    if (ABL_NOLD) return;
    if (s < 12) { if (pc == 0) { if (s <= 10) issue_x((s + 5) >> 1, (s + 5) & 1); } else issue_w1(s + 4, pc - 1); }
    else if (s == 12) w2_issue<0, 0>(p, c, pc, pc + 1);
    else if (s == 13) w2_issue<1, 1>(p, c, pc, pc + 1);
    else if (s == 14) w2_issue<2, 2>(p, c, pc, pc + 1);
    else w2_issue<3, 3>(p, c, pc, pc + 1);
  };
#pragma unroll
  for (int l = 0; l < NT; ++l) {
    o.acs[l] = f32x4{0, 0, 0, 0};
#pragma unroll
    for (int t = 0; t < 4; ++t) o.acc[l][t] = f32x4{0, 0, 0, 0};
  }
  o.s1 = 0.f; o.s2 = 0.f;
  const u32x4 ones = {0x3F803F80u, 0x3F803F80u, 0x3F803F80u, 0x3F803F80u};
  STAMP(14);
  SCHED();
  wait_bar<4>();
  asm volatile("" : "+s"(ep));
  ep += 0x9E3779B97F4A7C15ull;
  ep = (ep ^ (ep >> 30)) * 0xBF58476D1CE4E5B9ull;
  ep = (ep ^ (ep >> 27)) * 0x94D049BB133111EBull;
  c.tag = ep ^ (ep >> 31);
  STAMP(1);
  SCHED();
#pragma unroll
  for (int i = 0; i < 16; ++i) read1(0, i);
  SCHED();
#pragma unroll
  for (int s = 0; s < 16; ++s) {
    const int b = s & 1;
    if (s == 13) relane(c);
#pragma unroll
    for (int i = 0; i < 10; ++i) {
      mm(b, i);
      if (s > 0 && (i & 1) == 0 && i < 8) issue1(s - 1, i >> 1);
      if (s == 0 && i < 8) issue1(i < 4 ? -2 : -1, i & 3);
      if (STATPOS == 0) { if (i & 1) stat(b, i >> 1); if (i == 8) stat(b, 5); }
      if (STATPOS == 1) { if (i == 1 || i == 5 || i == 9) stat(b, i >> 2); }
      if (STATPOS == 2) { if (i == 8 || i == 9) stat(b, i - 8); }
      SCHED();
    }
    lgkm0();
    if (s == 15) wait_bar<63>();
    else if (s == 12 || s == 13) wait_bar<7>();
    else wait_bar<8>();
    if (s == 0) STAMP(27);
    if (s == 3) { STAMP(28); CSTAMP(29); }
    SCHED();
#pragma unroll
    for (int i = 0; i < 10; ++i) {
      mm(b, 10 + i);
      if (s < 15 && i < 8 && !(ABL_NORD && s > 0)) { read1(s + 1, 2 * i); read1(s + 1, 2 * i + 1); }
      if (STATPOS == 0) { if (i >= 8) stat(b, i - 2); }
      if (STATPOS == 1) { if (i & 1) stat(b, 3 + (i >> 1)); }
      if (STATPOS == 2) { if (i >= 4) stat(b, i - 2); }
      SCHED();
    }
    if (!ABL_NOCS && c.m == (u32)(s & 1)) {
#pragma unroll
      for (int l = 0; l < NT; ++l) {
        if (TA && l == 4) o.acs[l] = mfma16(ones, wf[b][l], o.acs[l]);
        else o.acs[l] = mfma16(wf[b][l], ones, o.acs[l]);
      }
    }
    SCHED();
  }
#pragma unroll
  for (int pc = 0; pc < 4; ++pc) issue1(15, pc);
  STAMP(26);
}

template <bool TA> DI void stats_exchange(const Ctx& c, u32 cx, P1<TA>& o, float (&rs)[4], float (&mr)[4]) {
  constexpr int NT = P1<TA>::NT;
  u32 a = asu(o.s1), b = asu(o.s2);
  swap32(a, b);
  float r = asf(a) + asf(b);
  a = asu(r); b = a;
  swap16(a, b);
  r = asf(a) + asf(b);
  a = asu(r); b = a;
  swap32(a, b);
  const float mean = asf(a) * (1.f / 512.f);
  const float var = __builtin_fmaxf(__builtin_fmaf(-mean, mean, asf(b) * (1.f / 512.f)), 0.f);
  const float rstd = __builtin_amdgcn_rsqf(var + 1e-5f);
  if (c.lane < 16) *(LAS f32x2*)(L_ST + (64 * c.m + 16 * cx + c.i16) * 8) = f32x2{rstd, mean * rstd};
  const u32 cb = L_CSP + ((c.m * 4 + c.n) * 6) * 64;
#pragma unroll
  for (int l = 0; l < NT; ++l) {
    if (TA && l == 4) { if (c.lane < 16) *(LAS float*)(cb + l * 64 + c.i16 * 4) = o.acs[l][0]; }
    else if (c.i16 == 0) *(LAS f32x4*)(cb + l * 64 + c.g * 16) = o.acs[l];
  }
  SCHED();
  lgkm_bar();
  SCHED();
#pragma unroll
  for (int s = 0; s < 4; ++s) {
    const f32x2 v = *(const LAS f32x2*)(L_ST + (64 * c.m + 16 * (s ^ cx) + c.i16) * 8);
    rs[s] = v.x; mr[s] = v.y;
  }
}
DI f32x4 cs_rows(const Ctx& c, int l) {
  return lds128f(L_CSP + (c.n * 6 + l) * 64 + c.g * 16) + lds128f(L_CSP + ((4 + c.n) * 6 + l) * 64 + c.g * 16);
}
DI u32x2 glu4(f32x4 ag, f32x4 au, f32x4 cg, f32x4 cu, float rs, float mr) {
  const float nrs = -LOG2E * rs, lmr = LOG2E * mr;
  float h[4];
#pragma unroll
  for (int r = 0; r < 4; ++r) {
    const float e = ex2(__builtin_fmaf(ag[r], nrs, lmr * cg[r]));
    const float u = __builtin_fmaf(au[r], rs, -(mr * cu[r]));
    h[r] = u * rcp(1.f + e);
  }
  return u32x2{pk2(h[0], h[1]), pk2(h[2], h[3])};
}
DI u32x2 ln4(f32x4 a, f32x4 cs, float rs, float mr) {
  float y[4];
#pragma unroll
  for (int r = 0; r < 4; ++r) y[r] = __builtin_fmaf(a[r], rs, -(mr * cs[r]));
  return u32x2{pk2(y[0], y[1]), pk2(y[2], y[3])};
}
DI u32x4 pair_slots(u32x2 x, u32x2 y) {
  u32 x0 = x.x, x1 = x.y, y0 = y.x, y1 = y.y;
  swap16(x0, y0); swap16(x1, y1);
  return u32x4{x0, x1, y0, y1};
}
DI void features(u32x4 aw, u32x4 b, u32x4 (&ph)[2]) {
  f32x16 z;
#pragma unroll
  for (int i = 0; i < 16; ++i) z[i] = 0.f;
  const f32x16 y = mfma32(aw, b, z);
  float mp = fmax2(fmax2(fmax2(y[0], y[1]), y[2]), fmax2(fmax2(y[3], y[4]), y[5]));
  mp = fmax2(fmax2(mp, y[6]), y[7]);
  float mn = fmax2(fmax2(fmax2(y[8], y[9]), y[10]), fmax2(fmax2(y[11], y[12]), y[13]));
  mn = fmax2(fmax2(mn, y[14]), y[15]);
  u32 a = asu(mp), bb = asu(mn);
  swap32(a, bb);
  a = asu(fmax2(asf(a), asf(bb)) * -LOG2E); bb = a;
  swap32(a, bb);
  float e[16];
#pragma unroll
  for (int i = 0; i < 8; ++i) { e[i] = ex2(__builtin_fmaf(y[i], LOG2E, asf(a))); e[8 + i] = ex2(__builtin_fmaf(y[8 + i], LOG2E, asf(bb))); }
  const float sp = ((e[0] + e[1]) + (e[2] + e[3])) + ((e[4] + e[5]) + (e[6] + e[7]));
  const float sn = ((e[8] + e[9]) + (e[10] + e[11])) + ((e[12] + e[13]) + (e[14] + e[15]));
  a = asu(sp); bb = asu(sn);
  swap32(a, bb);
  a = asu(rcp(asf(a) + asf(bb))); bb = a;
  swap32(a, bb);
  const float ip = asf(a), in = asf(bb);
#pragma unroll
  for (int i = 0; i < 4; ++i) { ph[0][i] = pk2(e[2 * i] * ip, e[2 * i + 1] * ip); ph[1][i] = pk2(e[8 + 2 * i] * in, e[9 + 2 * i] * in); }
}

DI u32 a_off(u32 tg, u32 wsl, u32 step, u32 tb, u32 lanep) { return WS_A + (((tg * 8 + wsl) * 9 + step) * 4 + tb) * 1024 + lanep * 16; }

DI void p2_load(rsrc_t rs, u32 vbase, int step, u32x4 (&f)[4]) {
#pragma unroll
  for (int t = 0; t < 4; ++t) f[t] = ld_sc1(rs, vbase + t * 1024, step * 4096);
}
DI void p2_mma(f32x4 (&acc)[4][4], const u32x4 (&wv)[4], const u32x4 (&af)[4]) {
#pragma unroll
  for (int t = 0; t < 4; ++t)
#pragma unroll
    for (int v = 0; v < 4; ++v) acc[v][t] = mfma16(wv[v], af[t], acc[v][t]);
}

template <bool TA> DI void block_body(const Params& p, Ctx& c, u32 off, u32* stamps) {
  const u32 cx = TA ? c.n : (c.n ^ ((c.q & 1) << 1));
  const rsrc_t rs_ws = __builtin_amdgcn_make_buffer_rsrc(p.ws, 0, WS_BYTES, 0x00020000);
  P1<TA> o;
  relane(c);
  main_loop<TA>(p, c, off, cx, o, stamps);
  STAMP(2);
  CSTAMP(30);
  relane(c);
  float rs[4], mr[4];
  stats_exchange<TA>(c, cx, o, rs, mr);
  u32x4 w2r[4][4];
  const u32 tgp = c.ti * 2 + c.m;
  const u32 rho = c.g;
  char* f2 = p.ws + WS_F2 + c.ti * 256;
  STAMP(16);
  SCHED();
  wait_vm<8>();
  STAMP(17);
  w2_read<0>(c, w2r[0]); w2_read<1>(c, w2r[1]);
  wait_lgkm();
  SCHED();
  w2_issue<4, 0>(p, c); w2_issue<5, 1>(p, c);
  STAMP(18);
  SCHED();
  u32x4 phq[2][2];
  if (TA) {
    u32x4 aw;
    {
      const u32 qq = c.i16 >> 2, pp = c.i16 & 3, h = c.lane >> 5;
      const u32 a = L_WPH + c.n * 512 + (8 * h + qq) * 32 + pp * 8;
      aw = ldstr2(a, a + 128);
      const u32 sg = (c.lane & 16) ? 0x80008000u : 0u;
      aw.x ^= sg; aw.y ^= sg; aw.z ^= sg; aw.w ^= sg;
    }
    const f32x4 csk = cs_rows(c, 1);
    const float csv = *(const LAS float*)(L_CSP + (c.n * 6 + 4) * 64 + c.i16 * 4) + *(const LAS float*)(L_CSP + ((4 + c.n) * 6 + 4) * 64 + c.i16 * 4);
    f32x16 kv;
#pragma unroll
    for (int i = 0; i < 16; ++i) kv[i] = 0.f;
    const u32 img = L_SCR + c.w * 2048;
    const u32 cone = c.i16 == 0 ? 0x3F803F80u : 0u;
#pragma unroll
    for (int hf = 0; hf < 2; ++hf) {
      const u32x4 kb = pair_slots(ln4(o.acc[1][2 * hf], csk, rs[2 * hf], mr[2 * hf]), ln4(o.acc[1][2 * hf + 1], csk, rs[2 * hf + 1], mr[2 * hf + 1]));
      u32x4 ph[2];
      features(aw, kb, ph);
      {
        const u32 t = c.lane & 31, h = c.lane >> 5, x = (t >> 1) & 7;
        if (hf) wait_lgkm();
#pragma unroll
        for (int G = 0; G < 4; ++G)
          *(LAS u32x2*)(img + t * 64 + (((2 * G + h) ^ x) << 3)) = u32x2{ph[G >> 1][2 * (G & 1)], ph[G >> 1][2 * (G & 1) + 1]};
      }
      wait_lgkm();
#pragma unroll
      for (int ks = 0; ks < 2; ++ks) {
        const int s = 2 * hf + ks;
        const u32 qq = c.i16 >> 2, pp = c.i16 & 3, blk = (c.lane >> 4) & 1, h = c.lane >> 5;
        const u32 r0 = 16 * ks + 8 * h + qq, r1 = r0 + 4;
        const u32x4 pa = ldstr2(img + r0 * 64 + (((4 * blk + pp) ^ ((r0 >> 1) & 7)) << 3), img + r1 * 64 + (((4 * blk + pp) ^ ((r1 >> 1) & 7)) << 3));
        const u32 sa = L_ST + (64 * c.m + 16 * (s ^ cx) + 4 * c.g) * 8;
        const f32x4 st0 = lds128f(sa), st1 = lds128f(sa + 16);
        const float v0 = __builtin_fmaf(o.acc[4][s][0], st0[0], -(st0[1] * csv)), v1 = __builtin_fmaf(o.acc[4][s][1], st0[2], -(st0[3] * csv));
        const float v2 = __builtin_fmaf(o.acc[4][s][2], st1[0], -(st1[1] * csv)), v3 = __builtin_fmaf(o.acc[4][s][3], st1[2], -(st1[3] * csv));
        const u32x4 vb = pair_slots(u32x2{pk2(v0, v1), pk2(v2, v3)}, u32x2{cone, cone});
        kv = mfma32(pa, vb, kv);
      }
      if (hf == 0) {
        STAMP(19);
        SCHED();
        wait_vm<8>();
        STAMP(20);
        w2_read<2>(c, w2r[2]); w2_read<3>(c, w2r[3]);
        wait_lgkm();
        SCHED();
        w2_issue<6, 2>(p, c); w2_issue<7, 3>(p, c);
        STAMP(21);
        SCHED();
      }
    }
    STAMP(22);
    {
      f32x4 s0, s1, k0, k1;
#pragma unroll
      for (int i = 0; i < 4; ++i) {
        s0[i] = c.m ? kv[i] : kv[8 + i]; s1[i] = c.m ? kv[4 + i] : kv[12 + i];
        k0[i] = c.m ? kv[8 + i] : kv[i]; k1[i] = c.m ? kv[12 + i] : kv[4 + i];
      }
      wait_lgkm();
      *(LAS f32x4*)(img + c.lane * 16) = s0;
      *(LAS f32x4*)(img + 1024 + c.lane * 16) = s1;
      SCHED();
      lgkm_bar();
      SCHED();
      const u32 pim = L_SCR + (c.w ^ 4) * 2048;
      k0 += lds128f(pim + c.lane * 16);
      k1 += lds128f(pim + 1024 + c.lane * 16);
      const u32x4 v = {pk2(k0[0], k0[1]), pk2(k0[2], k0[3]), pk2(k1[0], k1[1]), pk2(k1[2], k1[3])};
      const u32 d = c.lane & 31, h = c.lane >> 5, hd = c.q * 4 + c.n;
      const u32 vo = d < 16 ? WS_KVP + ((hd * 16 + c.ti) * 2 + c.m) * 512 + (h * 16 + d) * 16
                            : WS_ZP + (((hd * 2 + c.m) * 2 + h) * 16 + c.ti) * 16;
      if (d <= 16) st_sc1(v, rs_ws, vo, 0);
    }
    STAMP(3);
    SCHED();
    u32x4 hv[2];
    {
      const f32x4 csg = cs_rows(c, 2), csu = cs_rows(c, 3);
#pragma unroll
      for (int hf = 0; hf < 2; ++hf)
        hv[hf] = pair_slots(glu4(o.acc[2][2 * hf], o.acc[3][2 * hf], csg, csu, rs[2 * hf], mr[2 * hf]),
                            glu4(o.acc[2][2 * hf + 1], o.acc[3][2 * hf + 1], csg, csu, rs[2 * hf + 1], mr[2 * hf + 1]));
    }
    const f32x4 csq = cs_rows(c, 0);
    SCHED();
    wait_bar<0>();
    if (c.w == 0 && c.lane == 0) flag_st(p.ws + WS_KVF + (c.q * 16 + c.ti) * 8, c.tag);
    STAMP(4);
    SCHED();
#pragma unroll
    for (int hf = 0; hf < 2; ++hf)
      st_sc1(hv[hf], rs_ws, a_off(tgp, c.q, 5 + (c.n >> 1), (2 * hf + (rho & 1)) ^ cx, c.i16 + 16 * (2 * (c.n & 1) + (rho >> 1))), 0);
    SCHED();
#pragma unroll
    for (int hf = 0; hf < 2; ++hf) {
      const u32x4 qb = pair_slots(ln4(o.acc[0][2 * hf], csq, rs[2 * hf], mr[2 * hf]), ln4(o.acc[0][2 * hf + 1], csq, rs[2 * hf + 1], mr[2 * hf + 1]));
      features(aw, qb, phq[hf]);
    }
    SCHED();
    wait_bar<0>();
    if (c.w == 0 && c.lane == 0) flag_st(f2 + c.q * 8, c.tag);
    STAMP(5);
  } else {
    const f32x4 cg0 = cs_rows(c, 0), cu0 = cs_rows(c, 1), cg1 = cs_rows(c, 2), cu1 = cs_rows(c, 3);
    u32x4 hv[5];
#pragma unroll
    for (int s = 0; s < 4; ++s) {
      const u32x2 a = glu4(o.acc[0][s], o.acc[1][s], cg0, cu0, rs[s], mr[s]), b = glu4(o.acc[2][s], o.acc[3][s], cg1, cu1, rs[s], mr[s]);
      hv[s] = u32x4{a.x, a.y, b.x, b.y};
      if (s == 1) {
        STAMP(19);
        SCHED();
        wait_vm<8>();
        STAMP(20);
        w2_read<2>(c, w2r[2]); w2_read<3>(c, w2r[3]);
        wait_lgkm();
        SCHED();
        w2_issue<6, 2>(p, c); w2_issue<7, 3>(p, c);
        STAMP(21);
        SCHED();
      }
    }
    const f32x4 cgs = cs_rows(c, 4), cus = cs_rows(c, P1<TA>::NT - 1);
    hv[4] = pair_slots(glu4(o.acc[4][0], o.acc[P1<TA>::NT - 1][0], cgs, cus, rs[0], mr[0]), glu4(o.acc[4][1], o.acc[P1<TA>::NT - 1][1], cgs, cus, rs[1], mr[1]));
#pragma unroll
    for (int s = 0; s < 4; ++s) st_sc1(hv[s], rs_ws, a_off(tgp, c.q, c.n, s ^ cx, c.lane), 0);
    st_sc1(hv[4], rs_ws, a_off(tgp, (c.q & 6) + (c.n >> 1), 4, (rho & 1) ^ cx, c.i16 + 16 * (2 * (c.n & 1) + (rho >> 1))), 0);
    STAMP(3);
    SCHED();
    wait_bar<0>();
    if (c.w == 0 && c.lane == 0) flag_st(f2 + (8 + c.q) * 8, c.tag);
    STAMP(5);
    SCHED();
    w2_issue<8, 4>(p, c);
    SCHED();
  }

  relane(c);
  const char* f2c = p.ws + WS_F2 + (c.tg >> 1) * 256;
  const u32 abase = WS_A + (c.tg * 8 + c.w) * 9 * 4096 + c.lane * 16;
  u32x4 af[9][4];
  f32x4 acc[4][4];
#pragma unroll
  for (int v = 0; v < 4; ++v)
#pragma unroll
    for (int t = 0; t < 4; ++t) acc[v][t] = f32x4{0, 0, 0, 0};
  u32x4 kvl[8], zl[2];
  if (TA) {
    poll_tag(p.ws + WS_KVF + (c.q * 16 + c.i16) * 8, c.tag);
    STAMP(6);
    const u32 hd = c.q * 4 + c.n, u = c.lane & 31, ph = c.lane >> 5;
#pragma unroll
    for (int i = 0; i < 8; ++i) kvl[i] = ld_sc1(rs_ws, WS_KVP + ((hd * 16 + 8 * ph) * 2 + c.m) * 512 + u * 16, i * 1024);
#pragma unroll
    for (int s = 0; s < 2; ++s) zl[s] = ld_sc1(rs_ws, WS_ZP + (((hd * 2 + s) * 2 + ph) * 16 + c.i16) * 16, 0);
    SCHED();
  }
  {
    const u32 idx = (c.lane & 3) == 0 ? c.w : ((c.lane & 1) ? 8 + (c.w ^ 1) : 8 + c.w);
    poll_tag(f2c + idx * 8, c.tag);
  }
  STAMP(7);
#pragma unroll
  for (int s = 0; s < 5; ++s) p2_load(rs_ws, abase, s, af[s]);
  SCHED();
  if (TA) {
    float t[8];
#pragma unroll
    for (int j = 0; j < 8; ++j) t[j] = 0.f;
#pragma unroll
    for (int i = 0; i < 8; ++i)
#pragma unroll
      for (int j = 0; j < 4; ++j) { t[2 * j] += asf(kvl[i][j] << 16); t[2 * j + 1] += asf(kvl[i][j] & 0xffff0000u); }
    float r4[4];
#pragma unroll
    for (int j = 0; j < 4; ++j) { u32 a = asu(t[j]), b = asu(t[4 + j]); swap32(a, b); r4[j] = asf(a) + asf(b); }
    const u32 ex = L_SCR + (c.n * 2 + c.m) * 512;
    *(LAS u32x2*)(ex + (c.lane & 31) * 16 + (c.lane >> 5) * 8) = u32x2{pk2(r4[0], r4[1]), pk2(r4[2], r4[3])};
    SCHED();
    lgkm_bar();
    SCHED();
    u32x4 ka[2];
#pragma unroll
    for (int s = 0; s < 2; ++s) {
      const u32x4 kk = lds128(L_SCR + (c.n * 2 + s) * 512 + ((c.lane >> 5) * 16 + c.i16) * 16);
      const bool isz = c.lane & 16;
      ka[s] = u32x4{isz ? zl[s].x : kk.x, isz ? zl[s].y : kk.y, isz ? zl[s].z : kk.z, isz ? zl[s].w : kk.w};
    }
#pragma unroll
    for (int hf = 0; hf < 2; ++hf) {
      f32x16 y;
#pragma unroll
      for (int i = 0; i < 16; ++i) y[i] = 0.f;
      y = mfma32(ka[0], phq[hf][0], y);
      y = mfma32(ka[1], phq[hf][1], y);
      const float dn = ((y[8] + y[9]) + (y[10] + y[11])) + ((y[12] + y[13]) + (y[14] + y[15]));
      u32 a = asu(dn), b = a;
      swap32(a, b);
      const float inv = rcp(asf(a) + asf(b) + 1e-6f);
      const u32x4 ov = {pk2(y[0] * inv, y[1] * inv), pk2(y[2] * inv, y[3] * inv), pk2(y[4] * inv, y[5] * inv), pk2(y[6] * inv, y[7] * inv)};
      st_sc1(ov, rs_ws, a_off(tgp, c.q, 7 + (c.n >> 1), (2 * hf + (rho & 1)) ^ cx, c.i16 + 16 * (2 * (c.n & 1) + (rho >> 1))), 0);
    }
    STAMP(8);
    SCHED();
    p2_mma(acc, w2r[0], af[0]);
    p2_mma(acc, w2r[1], af[1]);
    SCHED();
    wait_bar<0>();
    if (c.w == 0 && c.lane == 0) flag_st(f2 + (16 + c.q) * 8, c.tag);
    STAMP(9);
    SCHED();
    w2_issue<8, 4>(p, c);
    p2_load(rs_ws, abase, 5, af[5]);
    p2_load(rs_ws, abase, 6, af[6]);
    SCHED();
  } else {
    p2_mma(acc, w2r[0], af[0]);
    SCHED();
    p2_load(rs_ws, abase, 5, af[5]);
    SCHED();
    p2_mma(acc, w2r[1], af[1]);
    SCHED();
    p2_load(rs_ws, abase, 6, af[6]);
    SCHED();
  }
  relane(c);
  const u32 tok = c.w * 8 + (c.lane >> 3), c8 = c.lane & 7;
  const size_t oo = (size_t)(c.tg * 64 + tok) * 1024 + c.cg * 128 + c8 * 16;
  const u32x4 xres = *(const GAS u32x4*)(p.x + oo);
  p2_mma(acc, w2r[2], af[2]);
  p2_mma(acc, w2r[3], af[3]);
  SCHED();
  poll_tag(f2c + (16 + c.w) * 8, c.tag);
  STAMP(10);
  p2_load(rs_ws, abase, 7, af[7]);
  p2_load(rs_ws, abase, 8, af[8]);
  SCHED();
  {
    u32x4 wv[4];
    w2_read<0>(c, wv); p2_mma(acc, wv, af[4]);
    w2_read<1>(c, wv); p2_mma(acc, wv, af[5]);
    w2_read<2>(c, wv); p2_mma(acc, wv, af[6]);
    w2_read<3>(c, wv); p2_mma(acc, wv, af[7]);
    w2_read<4>(c, wv); p2_mma(acc, wv, af[8]);
  }
  STAMP(11);
  SCHED();
  lgkm_bar();
  STAMP(23);
  SCHED();
#pragma unroll
  for (int v = 0; v < 4; ++v)
#pragma unroll
    for (int t = 0; t < 4; ++t)
      *(LAS f32x4*)(c.w * 16384 + (16 * t + c.i16) * 256 + (((4 * v + c.g) ^ (c.i16 & 7)) << 4)) = acc[v][t];
  SCHED();
  lgkm_bar();
  STAMP(24);
  SCHED();
  f32x4 r0 = {asf(xres.x << 16), asf(xres.x & 0xffff0000u), asf(xres.y << 16), asf(xres.y & 0xffff0000u)};
  f32x4 r1 = {asf(xres.z << 16), asf(xres.z & 0xffff0000u), asf(xres.w << 16), asf(xres.w & 0xffff0000u)};
#pragma unroll
  for (int ww = 0; ww < 8; ++ww) {
    r0 += lds128f(ww * 16384 + tok * 256 + (((2 * c8) ^ (tok & 7)) << 4));
    r1 += lds128f(ww * 16384 + tok * 256 + (((2 * c8 + 1) ^ (tok & 7)) << 4));
  }
  *(GAS u32x4*)(p.out + oo) = u32x4{pk2(r0[0], r0[1]), pk2(r0[2], r0[3]), pk2(r1[0], r1[1]), pk2(r1[2], r1[3])};
  STAMP(12);
  CSTAMP(31);
}

__global__ __launch_bounds__(512, 2) void fused_kernel(Params p) {
  __shared__ __attribute__((aligned(16))) char smem[163840];
  asm volatile("" ::"v"((LAS char*)smem));
  Ctx c;
  u32* stamps = nullptr;
  STAMP(0);
  c.lane = threadIdx.x & 63;
  c.w = __builtin_amdgcn_readfirstlane(threadIdx.x >> 6);
  c.m = c.w >> 2; c.n = c.w & 3; c.g = c.lane >> 4; c.i16 = c.lane & 15;
  const u32 b = blockIdx.x, xc = b & 7, r = b >> 3;
  c.ti = (xc & 1) * 8 + (r & 7);
  const u32 cjl = r >> 3;
  c.q = (xc >> 1) * 2 + (cjl & 1);
  c.tg = xc * 4 + (r & 3); c.cg = r >> 2;
  const u32 off = STAGGER ? ((2 * cjl + (r & 7)) & 7) : 0;
  {
    u64 pc; u32 len;
    asm volatile("s_getpc_b64 %0\n.Lfk_pc:\n\ts_mov_b32 %1, .Lfk_end-.Lfk_pc" : "=s"(pc), "=s"(len));
    u32 o = threadIdx.x * 128;
    o = o < len ? o : 0;
    asm volatile("s_mov_b32 m0, %2\n\ts_nop 0\n\tglobal_load_lds_dword %0, %1" ::"v"(o), "s"(pc), "s"(L_JUNK + c.w * 256) : "memory");
  }
  if (cjl < 2) {
    relane(c);
    if (c.w < 2) glds16s(p.wphi + c.q * 2048 + c.w * 1024, c.lane * 16, L_WPH + c.w * 1024);
    block_body<true>(p, c, off, stamps);
  } else {
    block_body<false>(p, c, off, stamps);
  }
  relane(c);
  if (b == 0 && c.w == 0) {
    const char* f = p.ws + WS_F2 + (c.lane >> 2) * 256 + (c.lane & 3) * 32;
    poll_tag(f, c.tag); poll_tag(f + 8, c.tag); poll_tag(f + 16, c.tag); poll_tag(f + 24, c.tag);
    if (c.lane == 0) flag_st(p.ws + WS_E, c.tag);
  }
  asm volatile(".Lfk_end:");
}

extern "C" void kernel_launch(void* const* d_in, const int* in_sizes, int n_in, void* d_out, int out_size, void* d_ws, size_t ws_size,
                              hipStream_t stream) {
  Params p;
  p.x = (const char*)d_in[0]; p.wqkv = (const char*)d_in[1]; p.wphi = (const char*)d_in[2]; p.wo = (const char*)d_in[3];
  p.wg = (const char*)d_in[5]; p.wu = (const char*)d_in[6]; p.wd = (const char*)d_in[7];
  p.out = (char*)d_out; p.ws = (char*)d_ws;
  fused_kernel<<<256, 512, 0, stream>>>(p);
}
```

```cpp
#include <hip/hip_runtime.h>
#include <stdint.h>
#pragma clang diagnostic ignored "-Wint-to-pointer-cast"

typedef unsigned u32;
typedef unsigned long long u64;
typedef __attribute__((ext_vector_type(4))) unsigned u32x4;
typedef __attribute__((ext_vector_type(2))) unsigned u32x2;
typedef __attribute__((ext_vector_type(2))) float f32x2;
typedef __attribute__((ext_vector_type(4))) float f32x4;
typedef __attribute__((ext_vector_type(16))) float f32x16;
typedef __attribute__((ext_vector_type(8))) __bf16 bf16x8;
typedef __attribute__((ext_vector_type(2))) __bf16 bf16x2;
typedef __attribute__((ext_vector_type(4))) short s16x4;
#define LAS __attribute__((address_space(3)))
#define GAS __attribute__((address_space(1)))
#define CAS __attribute__((address_space(4)))
#define DI __device__ __forceinline__

#define ABL_NOSWZ 0
#define ABL_NOCS 0
#define ABL_NOSTAT 0
#define ABL_NOLD 0
#define ABL_NORD 0
#define TLBW 1
#define STATPOS 0
#define STAGGER 1
#define CODE_PF 1

constexpr u32 WS_E = 0;
constexpr u32 WS_KVF = 256;
constexpr u32 WS_F2 = 2048;
constexpr u32 WS_KVP = 8192;
constexpr u32 WS_ZP = WS_KVP + 524288;
constexpr u32 WS_A = 1u << 20;
constexpr u32 WS_BYTES = WS_A + 32u * 72u * 4096u;
constexpr u32 WS_DBG = 16u << 20;

constexpr u32 L_X = 0;
constexpr u32 L_W = 49152;
constexpr u32 L_M = 147456;
constexpr u32 L_ST = L_M;
constexpr u32 L_CSP = L_M + 1024;
constexpr u32 L_WPH = L_M + 4096;
constexpr u32 L_JUNK = L_M + 6144;
constexpr u32 L_SCR = L_X + 16384;

constexpr float LOG2E = 1.4426950408889634f;

struct Params {
  const char* x; const char* wqkv; const char* wphi; const char* wo;
  const char* wg; const char* wu; const char* wd;
  char* out; char* ws;
};

#define SCHED() __builtin_amdgcn_sched_barrier(0)
template <int N> DI void wait_vm() { asm volatile("s_waitcnt vmcnt(%0)" ::"n"(N) : "memory"); }
template <int N> DI void wait_bar() { asm volatile("s_waitcnt vmcnt(%0) lgkmcnt(0)\n\ts_barrier" ::"n"(N) : "memory"); }
#define PRO_BAR() asm volatile("s_barrier" ::: "memory")
DI void lgkm0() { __builtin_amdgcn_s_waitcnt(0xC07F); }
DI void lgkm_bar() { asm volatile("s_waitcnt lgkmcnt(0)\n\ts_barrier" ::: "memory"); }
DI void wait_lgkm() { asm volatile("s_waitcnt lgkmcnt(0)" ::: "memory"); }

DI void glds16(const char* g, u32 lds) {
  asm volatile("s_mov_b32 m0, %1\n\ts_nop 0\n\tglobal_load_lds_dwordx4 %0, off" ::"v"(g), "s"(lds) : "memory");
}
DI void glds16o128(const char* g, u32 lds) {
  asm volatile("s_mov_b32 m0, %1\n\ts_nop 0\n\tglobal_load_lds_dwordx4 %0, off offset:128" ::"v"(g), "s"(lds - 128) : "memory");
}
DI void glds16s(const char* sbase, u32 voff, u32 lds) {
  asm volatile("s_mov_b32 m0, %2\n\ts_nop 0\n\tglobal_load_lds_dwordx4 %0, %1" ::"v"(voff), "s"(sbase), "s"(lds) : "memory");
}
DI u32x4 lds128(u32 a) { return *(const LAS u32x4*)(a); }
DI f32x4 lds128f(u32 a) { return *(const LAS f32x4*)(a); }
DI u32x2 ldstr(u32 a) { return __builtin_bit_cast(u32x2, __builtin_amdgcn_ds_read_tr16_b64_v4i16((LAS s16x4*)(a))); }
DI u32x4 ldstr2(u32 a0, u32 a1) { u32x2 lo = ldstr(a0), hi = ldstr(a1); return u32x4{lo.x, lo.y, hi.x, hi.y}; }
DI f32x4 mfma16(u32x4 a, u32x4 b, f32x4 c) {
  return __builtin_amdgcn_mfma_f32_16x16x32_bf16(__builtin_bit_cast(bf16x8, a), __builtin_bit_cast(bf16x8, b), c, 0, 0, 0);
}
DI f32x16 mfma32(u32x4 a, u32x4 b, f32x16 c) {
  return __builtin_amdgcn_mfma_f32_32x32x16_bf16(__builtin_bit_cast(bf16x8, a), __builtin_bit_cast(bf16x8, b), c, 0, 0, 0);
}
DI u32 pk2(float a, float b) { bf16x2 v = {(__bf16)a, (__bf16)b}; return __builtin_bit_cast(u32, v); }
DI float asf(u32 x) { return __builtin_bit_cast(float, x); }
DI u32 asu(float x) { return __builtin_bit_cast(u32, x); }
DI float fmax2(float a, float b) { return __builtin_elementwise_maximum(a, b); }
DI void swap16(u32& a, u32& b) { auto r = __builtin_amdgcn_permlane16_swap(a, b, false, false); a = r[0]; b = r[1]; }
DI void swap32(u32& a, u32& b) { auto r = __builtin_amdgcn_permlane32_swap(a, b, false, false); a = r[0]; b = r[1]; }
DI float ex2(float x) { return __builtin_amdgcn_exp2f(x); }
DI float rcp(float x) { return __builtin_amdgcn_rcpf(x); }

typedef __amdgpu_buffer_rsrc_t rsrc_t;
DI u32x4 ld_sc1(rsrc_t rs, u32 voff, u32 soff) { return __builtin_amdgcn_raw_buffer_load_b128(rs, voff, soff, 16); }
DI void st_sc1(u32x4 v, rsrc_t rs, u32 voff, u32 soff) { __builtin_amdgcn_raw_buffer_store_b128(v, rs, voff, soff, 16); }
DI u64 flag_ld(const char* p) { return __hip_atomic_load((GAS u64*)p, __ATOMIC_RELAXED, __HIP_MEMORY_SCOPE_AGENT); }
DI void flag_st(char* p, u64 v) { __hip_atomic_store((GAS u64*)p, v, __ATOMIC_RELAXED, __HIP_MEMORY_SCOPE_AGENT); }
DI void poll_tag(const char* p, u64 tag) {
#pragma unroll 1
  for (u32 spins = 0; spins < (1u << 20); ++spins) {
    u64 v = flag_ld(p);
    if (__all(v == tag)) break;
    __builtin_amdgcn_s_sleep(1);
  }
  __builtin_amdgcn_fence(__ATOMIC_ACQUIRE, "wavefront");
}

#define STAMP(i) do {} while (0)
#define CSTAMP(i) do {} while (0)

struct Ctx {
  u32 w, m, n, lane, g, i16;
  u32 ti, q, cg, tg;
  u64 tag;
};

DI void relane(Ctx& c) {
  u32 l = __builtin_amdgcn_mbcnt_hi(~0u, __builtin_amdgcn_mbcnt_lo(~0u, 0u));
  asm volatile("" : "+v"(l));
  c.lane = l; c.g = l >> 4; c.i16 = l & 15;
}
DI u32 w2_area_piece(u32 area, u32 piece, u32 w) {
  if (area < 4) return piece < 3 ? L_W + area * 24576 + w * 3072 + piece * 1024
                                 : L_X + (area < 2 ? 32768 : 0) + (area & 1) * 8192 + w * 1024;
  return piece < 2 ? L_SCR + w * 2048 + piece * 1024 : L_M + w * 2048 + (piece - 2) * 1024;
}
template <int STEP, int AREA> DI void w2_issue(const Params& p, const Ctx& c, int g0 = 0, int g1 = 4) {
  const u32 j = c.lane >> 3;
  const u32 rowl = STEP < 4 ? 64 * (j >> 2) + (j & 3) : (STEP < 7 ? j : (j & 3) + 8 * (j >> 2));
  const u32 P0 = 8 + 5 * (c.w >> 1) + 2 * (c.w & 1), PS = 8 + 5 * (c.w >> 1) + 4;
#pragma unroll
  for (int g = g0; g < g1; ++g) {
    const u32 sw = (((j >> 1) & 1) << 1) | ((g & 1) << 2);
    const u32 voff = rowl * 1024 + (((c.lane & 7) ^ sw) << 4);
    u32 rows;
    if (STEP < 4) rows = 64 * P0 + 16 * STEP + 4 * g;
    else if (STEP == 4) rows = 64 * PS + 32 * (c.w & 1) + 8 * g;
    else if (STEP < 7) rows = 64 * c.w + 32 * (STEP - 5) + 8 * g;
    else rows = 64 * c.w + 16 * (2 * (STEP - 7) + (g >> 1)) + 4 * (g & 1);
    const char* base = (STEP < 7 ? p.wd : p.wo) + (size_t)(rows * 1024 + c.cg * 128);
    glds16s(base, voff, w2_area_piece(AREA, g, c.w));
  }
}
template <int AREA> DI void w2_read(const Ctx& c, u32x4 (&f)[4]) {
  const u32 qq = c.i16 >> 2, pp = c.i16 & 3;
  u32 pb;
  if (AREA < 4) pb = c.g < 3 ? L_W + AREA * 24576 + c.w * 3072 + c.g * 1024 : L_X + (AREA < 2 ? 32768 : 0) + (AREA & 1) * 8192 + c.w * 1024;
  else pb = c.g < 2 ? L_SCR + c.w * 2048 + c.g * 1024 : L_M + c.w * 2048 + (c.g - 2) * 1024;
  const u32 sw = (((qq >> 1) & 1) << 1) | ((c.g & 1) << 2);
  const u32 a = pb + qq * 128 + (((pp >> 1) ^ sw) << 4) + 8 * (pp & 1);
#pragma unroll
  for (int v = 0; v < 4; ++v) f[v] = ldstr2((a ^ (v * 32)), (a ^ (v * 32)) + 512);
}

template <bool TA> struct P1 {
  static constexpr int NT = TA ? 5 : 6;
  f32x4 acc[NT][4];
  f32x4 acs[NT];
  float s1, s2;
};

template <bool TA> DI void main_loop(const Params& p, Ctx& c, u32 off, u32 cx, P1<TA>& o, u32* stamps) {
  constexpr int NT = P1<TA>::NT;
  const u32 lane = c.lane, w = c.w;
  const char* xs = p.x + (size_t)c.ti * 131072;
  u32 xv[2];
#pragma unroll
  for (int pc = 0; pc < 2; ++pc) xv[pc] = (16 * w + 8 * pc + (lane >> 3)) * 1024 + (((lane & 7) ^ (ABL_NOSWZ ? 0 : (4 * pc + (lane >> 4)))) << 4);
  const char* wsrc[3];
  {
    const u32 r = (lane >> 3) & 3, hi = lane >> 5;
    const u32 sw = (((r >> 1) & 1) << 1) | (((w >> 1) & 1) << 2);
    const u32 ch = (((lane & 7) ^ (ABL_NOSWZ ? 0 : sw)) << 4);
    const u32 kr = 4 * w + r;
    if (TA) {
      wsrc[0] = p.wqkv + (kr * 3072 + hi * 1024 + c.q * 128 + ch);
      wsrc[1] = (hi ? p.wu : p.wg) + (kr * 3584 + c.q * 128 + ch);
      wsrc[2] = p.wqkv + (kr * 3072 + 2048 + c.q * 128 + ch);
    } else {
      const u32 P0 = 8 + 5 * (c.q >> 1) + 2 * (c.q & 1), PS = 8 + 5 * (c.q >> 1) + 4;
      const char* b = (hi ? p.wu : p.wg) + (kr * 3584 + ch);
      wsrc[0] = b + P0 * 128; wsrc[1] = b + (P0 + 1) * 128; wsrc[2] = b + PS * 128;
    }
  }
  auto issue_x = [&](int c8, int pc) {
    const u32 kc = (c8 + off) & 7;
    glds16s(xs + kc * 128, xv[pc], L_X + (c8 % 3) * 16384 + (16 * w + 8 * pc) * 128);
  };
  auto issue_w1 = [&](int s, int pc) {
    const u32 k0 = (((s >> 1) + off) & 7) * 64 + (s & 1) * 32;
    const u32 dst = L_W + (s & 3) * 24576 + w * 3072;
    if (TA) {
      if (pc == 0) glds16(wsrc[0] + (size_t)(k0 * 3072), dst);
      else if (pc == 1) glds16(wsrc[1] + (size_t)(k0 * 3584), dst + 1024);
      else if (lane < 32) glds16(wsrc[2] + (size_t)(k0 * 3072), dst + 2048);
    } else {
      if (pc == 0) glds16(wsrc[0] + (size_t)(k0 * 3584), dst);
      else if (pc == 1) glds16o128(wsrc[0] + (size_t)(k0 * 3584), dst + 1024);
      else glds16(wsrc[2] + (size_t)(k0 * 3584), dst + 2048);
    }
  };
  auto issue_w = [&](int s) { issue_w1(s, 0); issue_w1(s, 1); issue_w1(s, 2); };
  STAMP(13);
  u64 ep;
  asm volatile("s_load_dwordx2 %0, %1, 0x0" : "=s"(ep) : "s"(p.ws + WS_E) : "memory");
  issue_x(0, 0); issue_x(0, 1); issue_w(0);
  STAMP(25);
  PRO_BAR();
  issue_x(1, 0); issue_w(1);
  u32 xa[2][4];
#pragma unroll
  for (int s = 0; s < 4; ++s) {
    const u32 row = 64 * c.m + 16 * (s ^ cx) + c.i16;
    xa[0][s] = L_X + row * 128 + ((c.g ^ (c.i16 >> 1)) << 4);
    xa[1][s] = xa[0][s] ^ 64;
  }
  u32 wa;
  {
    const u32 qq = c.i16 >> 2, pp = c.i16 & 3;
    const u32 sw = (((qq >> 1) & 1) << 1) | ((c.g & 1) << 2);
    wa = L_W + 2 * c.g * 3072 + qq * 128 + (((2 * c.n + (pp >> 1)) ^ sw) << 4) + 8 * (pp & 1);
  }
  u32 wb = wa + 49152;
  asm volatile("" : "+v"(wa), "+v"(wb));
  u32x4 xf[2][4], wf[2][NT];
  u32x2 wh[2][NT][2];
  auto read1 = [&](int s, int i) {
    const int b = s & 1;
    if (i >= 2 && i < 6) { xf[b][i - 2] = lds128(xa[s & 1][i - 2] + ((s >> 1) % 3) * 16384); return; }
    const int k = i < 2 ? i : i - 4, l = k >> 1, t = k & 1;
    if (l >= NT) return;
    wh[b][l][t] = ldstr(((s & 2) ? wb : wa) + (s & 1) * 24576 + l * 512 + t * 3072);
    if (t) wf[b][l] = u32x4{wh[b][l][0].x, wh[b][l][0].y, wh[b][l][1].x, wh[b][l][1].y};
  };
  auto mm = [&](int b, int i) {
    if (i < 16) { o.acc[i >> 2][i & 3] = mfma16(wf[b][i >> 2], xf[b][i & 3], o.acc[i >> 2][i & 3]); return; }
    if (TA) o.acc[4][i & 3] = mfma16(xf[b][i & 3], wf[b][4], o.acc[4][i & 3]);
    else { const int l = 4 + ((i >> 1) & 1) * (NT - 5), t = i & 1; o.acc[l][t] = mfma16(wf[b][l], xf[b][t], o.acc[l][t]); }
  };
  auto stat = [&](int b, int u) {
    if (ABL_NOSTAT) return;
    const u32 d = xf[b][0][u >> 1];
    const float v = asf((u & 1) ? (d & 0xffff0000u) : (d << 16));
    o.s1 += v; o.s2 = __builtin_fmaf(v, v, o.s2);
    asm volatile("" : "+v"(o.s1), "+v"(o.s2));
  };
  auto issue1 = [&](int s, int pc) {
    if (ABL_NOLD) return;
    if (s < 12) { if (pc == 0) { if (s <= 10) issue_x((s + 5) >> 1, (s + 5) & 1); } else issue_w1(s + 4, pc - 1); }
    else if (s == 12) w2_issue<0, 0>(p, c, pc, pc + 1);
    else if (s == 13) w2_issue<1, 1>(p, c, pc, pc + 1);
    else if (s == 14) w2_issue<2, 2>(p, c, pc, pc + 1);
    else w2_issue<3, 3>(p, c, pc, pc + 1);
  };
#pragma unroll
  for (int l = 0; l < NT; ++l) {
    o.acs[l] = f32x4{0, 0, 0, 0};
#pragma unroll
    for (int t = 0; t < 4; ++t) o.acc[l][t] = f32x4{0, 0, 0, 0};
  }
  o.s1 = 0.f; o.s2 = 0.f;
  const u32x4 ones = {0x3F803F80u, 0x3F803F80u, 0x3F803F80u, 0x3F803F80u};
  STAMP(14);
  SCHED();
  wait_bar<4>();
  asm volatile("" : "+s"(ep));
  ep += 0x9E3779B97F4A7C15ull;
  ep = (ep ^ (ep >> 30)) * 0xBF58476D1CE4E5B9ull;
  ep = (ep ^ (ep >> 27)) * 0x94D049BB133111EBull;
  c.tag = ep ^ (ep >> 31);
  STAMP(1);
  SCHED();
#pragma unroll
  for (int i = 0; i < 16; ++i) read1(0, i);
  SCHED();
#pragma unroll
  for (int s = 0; s < 16; ++s) {
    const int b = s & 1;
    if (s == 13) relane(c);
#pragma unroll
    for (int i = 0; i < 10; ++i) {
      mm(b, i);
      if (s > 0 && (i & 1) == 0 && i < 8) issue1(s - 1, i >> 1);
      if (s == 0 && i < 8) issue1(i < 4 ? -2 : -1, i & 3);
      if (STATPOS == 0) { if (i & 1) stat(b, i >> 1); if (i == 8) stat(b, 5); }
      if (STATPOS == 1) { if (i == 1 || i == 5 || i == 9) stat(b, i >> 2); }
      if (STATPOS == 2) { if (i == 8 || i == 9) stat(b, i - 8); }
      SCHED();
    }
    lgkm0();
    if (s == 15) wait_bar<63>();
    else if (s == 12 || s == 13) wait_bar<7>();
    else wait_bar<8>();
    if (s == 0) STAMP(27);
    if (s == 3) { STAMP(28); CSTAMP(29); }
    SCHED();
#pragma unroll
    for (int i = 0; i < 10; ++i) {
      mm(b, 10 + i);
      if (s < 15 && i < 8 && !(ABL_NORD && s > 0)) { read1(s + 1, 2 * i); read1(s + 1, 2 * i + 1); }
      if (STATPOS == 0) { if (i >= 8) stat(b, i - 2); }
      if (STATPOS == 1) { if (i & 1) stat(b, 3 + (i >> 1)); }
      if (STATPOS == 2) { if (i >= 4) stat(b, i - 2); }
      SCHED();
    }
    if (!ABL_NOCS && c.m == (u32)(s & 1)) {
#pragma unroll
      for (int l = 0; l < NT; ++l) {
        if (TA && l == 4) o.acs[l] = mfma16(ones, wf[b][l], o.acs[l]);
        else o.acs[l] = mfma16(wf[b][l], ones, o.acs[l]);
      }
    }
    SCHED();
  }
#pragma unroll
  for (int pc = 0; pc < 4; ++pc) issue1(15, pc);
  STAMP(26);
}

template <bool TA> DI void stats_exchange(const Ctx& c, u32 cx, P1<TA>& o, float (&rs)[4], float (&mr)[4]) {
  constexpr int NT = P1<TA>::NT;
  u32 a = asu(o.s1), b = asu(o.s2);
  swap32(a, b);
  float r = asf(a) + asf(b);
  a = asu(r); b = a;
  swap16(a, b);
  r = asf(a) + asf(b);
  a = asu(r); b = a;
  swap32(a, b);
  const float mean = asf(a) * (1.f / 512.f);
  const float var = __builtin_fmaxf(__builtin_fmaf(-mean, mean, asf(b) * (1.f / 512.f)), 0.f);
  const float rstd = __builtin_amdgcn_rsqf(var + 1e-5f);
  if (c.lane < 16) *(LAS f32x2*)(L_ST + (64 * c.m + 16 * cx + c.i16) * 8) = f32x2{rstd, mean * rstd};
  const u32 cb = L_CSP + ((c.m * 4 + c.n) * 6) * 64;
#pragma unroll
  for (int l = 0; l < NT; ++l) {
    if (TA && l == 4) { if (c.lane < 16) *(LAS float*)(cb + l * 64 + c.i16 * 4) = o.acs[l][0]; }
    else if (c.i16 == 0) *(LAS f32x4*)(cb + l * 64 + c.g * 16) = o.acs[l];
  }
  SCHED();
  lgkm_bar();
  SCHED();
#pragma unroll
  for (int s = 0; s < 4; ++s) {
    const f32x2 v = *(const LAS f32x2*)(L_ST + (64 * c.m + 16 * (s ^ cx) + c.i16) * 8);
    rs[s] = v.x; mr[s] = v.y;
  }
}
DI f32x4 cs_rows(const Ctx& c, int l) {
  return lds128f(L_CSP + (c.n * 6 + l) * 64 + c.g * 16) + lds128f(L_CSP + ((4 + c.n) * 6 + l) * 64 + c.g * 16);
}
DI u32x2 glu4(f32x4 ag, f32x4 au, f32x4 cg, f32x4 cu, float rs, float mr) {
  const float nrs = -LOG2E * rs, lmr = LOG2E * mr;
  float h[4];
#pragma unroll
  for (int r = 0; r < 4; ++r) {
    const float e = ex2(__builtin_fmaf(ag[r], nrs, lmr * cg[r]));
    const float u = __builtin_fmaf(au[r], rs, -(mr * cu[r]));
    h[r] = u * rcp(1.f + e);
  }
  return u32x2{pk2(h[0], h[1]), pk2(h[2], h[3])};
}
DI u32x2 ln4(f32x4 a, f32x4 cs, float rs, float mr) {
  float y[4];
#pragma unroll
  for (int r = 0; r < 4; ++r) y[r] = __builtin_fmaf(a[r], rs, -(mr * cs[r]));
  return u32x2{pk2(y[0], y[1]), pk2(y[2], y[3])};
}
DI u32x4 pair_slots(u32x2 x, u32x2 y) {
  u32 x0 = x.x, x1 = x.y, y0 = y.x, y1 = y.y;
  swap16(x0, y0); swap16(x1, y1);
  return u32x4{x0, x1, y0, y1};
}
DI void features(u32x4 aw, u32x4 b, u32x4 (&ph)[2]) {
  f32x16 z;
#pragma unroll
  for (int i = 0; i < 16; ++i) z[i] = 0.f;
  const f32x16 y = mfma32(aw, b, z);
  float mp = fmax2(fmax2(fmax2(y[0], y[1]), y[2]), fmax2(fmax2(y[3], y[4]), y[5]));
  mp = fmax2(fmax2(mp, y[6]), y[7]);
  float mn = fmax2(fmax2(fmax2(y[8], y[9]), y[10]), fmax2(fmax2(y[11], y[12]), y[13]));
  mn = fmax2(fmax2(mn, y[14]), y[15]);
  u32 a = asu(mp), bb = asu(mn);
  swap32(a, bb);
  a = asu(fmax2(asf(a), asf(bb)) * -LOG2E); bb = a;
  swap32(a, bb);
  float e[16];
#pragma unroll
  for (int i = 0; i < 8; ++i) { e[i] = ex2(__builtin_fmaf(y[i], LOG2E, asf(a))); e[8 + i] = ex2(__builtin_fmaf(y[8 + i], LOG2E, asf(bb))); }
  const float sp = ((e[0] + e[1]) + (e[2] + e[3])) + ((e[4] + e[5]) + (e[6] + e[7]));
  const float sn = ((e[8] + e[9]) + (e[10] + e[11])) + ((e[12] + e[13]) + (e[14] + e[15]));
  a = asu(sp); bb = asu(sn);
  swap32(a, bb);
  a = asu(rcp(asf(a) + asf(bb))); bb = a;
  swap32(a, bb);
  const float ip = asf(a), in = asf(bb);
#pragma unroll
  for (int i = 0; i < 4; ++i) { ph[0][i] = pk2(e[2 * i] * ip, e[2 * i + 1] * ip); ph[1][i] = pk2(e[8 + 2 * i] * in, e[9 + 2 * i] * in); }
}

DI u32 a_off(u32 tg, u32 wsl, u32 step, u32 tb, u32 lanep) { return WS_A + (((tg * 8 + wsl) * 9 + step) * 4 + tb) * 1024 + lanep * 16; }

DI void p2_load(rsrc_t rs, u32 vbase, int step, u32x4 (&f)[4]) {
#pragma unroll
  for (int t = 0; t < 4; ++t) f[t] = ld_sc1(rs, vbase + t * 1024, step * 4096);
}
DI void p2_mma(f32x4 (&acc)[4][4], const u32x4 (&wv)[4], const u32x4 (&af)[4]) {
#pragma unroll
  for (int t = 0; t < 4; ++t)
#pragma unroll
    for (int v = 0; v < 4; ++v) acc[v][t] = mfma16(wv[v], af[t], acc[v][t]);
}

template <bool TA> DI void block_body(const Params& p, Ctx& c, u32 off, u32* stamps) {
  const u32 cx = TA ? c.n : (c.n ^ ((c.q & 1) << 1));
  const rsrc_t rs_ws = __builtin_amdgcn_make_buffer_rsrc(p.ws, 0, WS_BYTES, 0x00020000);
  P1<TA> o;
  relane(c);
  main_loop<TA>(p, c, off, cx, o, stamps);
  STAMP(2);
  CSTAMP(30);
  relane(c);
  float rs[4], mr[4];
  stats_exchange<TA>(c, cx, o, rs, mr);
  u32x4 w2r[4][4];
  const u32 tgp = c.ti * 2 + c.m;
  const u32 rho = c.g;
  char* f2 = p.ws + WS_F2 + c.ti * 256;
  STAMP(16);
  SCHED();
  wait_vm<8>();
  STAMP(17);
  w2_read<0>(c, w2r[0]); w2_read<1>(c, w2r[1]);
  wait_lgkm();
  SCHED();
  w2_issue<4, 0>(p, c); w2_issue<5, 1>(p, c);
  STAMP(18);
  SCHED();
  u32x4 phq[2][2];
  if (TA) {
    u32x4 aw;
    {
      const u32 qq = c.i16 >> 2, pp = c.i16 & 3, h = c.lane >> 5;
      const u32 a = L_WPH + c.n * 512 + (8 * h + qq) * 32 + pp * 8;
      aw = ldstr2(a, a + 128);
      const u32 sg = (c.lane & 16) ? 0x80008000u : 0u;
      aw.x ^= sg; aw.y ^= sg; aw.z ^= sg; aw.w ^= sg;
    }
    const f32x4 csk = cs_rows(c, 1);
    const float csv = *(const LAS float*)(L_CSP + (c.n * 6 + 4) * 64 + c.i16 * 4) + *(const LAS float*)(L_CSP + ((4 + c.n) * 6 + 4) * 64 + c.i16 * 4);
    f32x16 kv;
#pragma unroll
    for (int i = 0; i < 16; ++i) kv[i] = 0.f;
    const u32 img = L_SCR + c.w * 2048;
    const u32 cone = c.i16 == 0 ? 0x3F803F80u : 0u;
#pragma unroll
    for (int hf = 0; hf < 2; ++hf) {
      const u32x4 kb = pair_slots(ln4(o.acc[1][2 * hf], csk, rs[2 * hf], mr[2 * hf]), ln4(o.acc[1][2 * hf + 1], csk, rs[2 * hf + 1], mr[2 * hf + 1]));
      u32x4 ph[2];
      features(aw, kb, ph);
      {
        const u32 t = c.lane & 31, h = c.lane >> 5, x = (t >> 1) & 7;
        if (hf) wait_lgkm();
#pragma unroll
        for (int G = 0; G < 4; ++G)
          *(LAS u32x2*)(img + t * 64 + (((2 * G + h) ^ x) << 3)) = u32x2{ph[G >> 1][2 * (G & 1)], ph[G >> 1][2 * (G & 1) + 1]};
      }
      wait_lgkm();
#pragma unroll
      for (int ks = 0; ks < 2; ++ks) {
        const int s = 2 * hf + ks;
        const u32 qq = c.i16 >> 2, pp = c.i16 & 3, blk = (c.lane >> 4) & 1, h = c.lane >> 5;
        const u32 r0 = 16 * ks + 8 * h + qq, r1 = r0 + 4;
        const u32x4 pa = ldstr2(img + r0 * 64 + (((4 * blk + pp) ^ ((r0 >> 1) & 7)) << 3), img + r1 * 64 + (((4 * blk + pp) ^ ((r1 >> 1) & 7)) << 3));
        const u32 sa = L_ST + (64 * c.m + 16 * (s ^ cx) + 4 * c.g) * 8;
        const f32x4 st0 = lds128f(sa), st1 = lds128f(sa + 16);
        const float v0 = __builtin_fmaf(o.acc[4][s][0], st0[0], -(st0[1] * csv)), v1 = __builtin_fmaf(o.acc[4][s][1], st0[2], -(st0[3] * csv));
        const float v2 = __builtin_fmaf(o.acc[4][s][2], st1[0], -(st1[1] * csv)), v3 = __builtin_fmaf(o.acc[4][s][3], st1[2], -(st1[3] * csv));
        const u32x4 vb = pair_slots(u32x2{pk2(v0, v1), pk2(v2, v3)}, u32x2{cone, cone});
        kv = mfma32(pa, vb, kv);
      }
      if (hf == 0) {
        STAMP(19);
        SCHED();
        wait_vm<8>();
        STAMP(20);
        w2_read<2>(c, w2r[2]); w2_read<3>(c, w2r[3]);
        wait_lgkm();
        SCHED();
        w2_issue<6, 2>(p, c); w2_issue<7, 3>(p, c);
        STAMP(21);
        SCHED();
      }
    }
    STAMP(22);
    {
      f32x4 s0, s1, k0, k1;
#pragma unroll
      for (int i = 0; i < 4; ++i) {
        s0[i] = c.m ? kv[i] : kv[8 + i]; s1[i] = c.m ? kv[4 + i] : kv[12 + i];
        k0[i] = c.m ? kv[8 + i] : kv[i]; k1[i] = c.m ? kv[12 + i] : kv[4 + i];
      }
      wait_lgkm();
      *(LAS f32x4*)(img + c.lane * 16) = s0;
      *(LAS f32x4*)(img + 1024 + c.lane * 16) = s1;
      SCHED();
      lgkm_bar();
      SCHED();
      const u32 pim = L_SCR + (c.w ^ 4) * 2048;
      k0 += lds128f(pim + c.lane * 16);
      k1 += lds128f(pim + 1024 + c.lane * 16);
      const u32x4 v = {pk2(k0[0], k0[1]), pk2(k0[2], k0[3]), pk2(k1[0], k1[1]), pk2(k1[2], k1[3])};
      const u32 d = c.lane & 31, h = c.lane >> 5, hd = c.q * 4 + c.n;
      const u32 vo = d < 16 ? WS_KVP + ((hd * 16 + c.ti) * 2 + c.m) * 512 + (h * 16 + d) * 16
                            : WS_ZP + (((hd * 2 + c.m) * 2 + h) * 16 + c.ti) * 16;
      if (d <= 16) st_sc1(v, rs_ws, vo, 0);
    }
    STAMP(3);
    SCHED();
    u32x4 hv[2];
    {
      const f32x4 csg = cs_rows(c, 2), csu = cs_rows(c, 3);
#pragma unroll
      for (int hf = 0; hf < 2; ++hf)
        hv[hf] = pair_slots(glu4(o.acc[2][2 * hf], o.acc[3][2 * hf], csg, csu, rs[2 * hf], mr[2 * hf]),
                            glu4(o.acc[2][2 * hf + 1], o.acc[3][2 * hf + 1], csg, csu, rs[2 * hf + 1], mr[2 * hf + 1]));
    }
    const f32x4 csq = cs_rows(c, 0);
    SCHED();
    wait_bar<0>();
    if (c.w == 0 && c.lane == 0) flag_st(p.ws + WS_KVF + (c.q * 16 + c.ti) * 8, c.tag);
    STAMP(4);
    SCHED();
#pragma unroll
    for (int hf = 0; hf < 2; ++hf)
      st_sc1(hv[hf], rs_ws, a_off(tgp, c.q, 5 + (c.n >> 1), (2 * hf + (rho & 1)) ^ cx, c.i16 + 16 * (2 * (c.n & 1) + (rho >> 1))), 0);
    SCHED();
#pragma unroll
    for (int hf = 0; hf < 2; ++hf) {
      const u32x4 qb = pair_slots(ln4(o.acc[0][2 * hf], csq, rs[2 * hf], mr[2 * hf]), ln4(o.acc[0][2 * hf + 1], csq, rs[2 * hf + 1], mr[2 * hf + 1]));
      features(aw, qb, phq[hf]);
    }
    SCHED();
    wait_bar<0>();
    if (c.w == 0 && c.lane == 0) flag_st(f2 + c.q * 8, c.tag);
    STAMP(5);
  } else {
    const f32x4 cg0 = cs_rows(c, 0), cu0 = cs_rows(c, 1), cg1 = cs_rows(c, 2), cu1 = cs_rows(c, 3);
    u32x4 hv[5];
#pragma unroll
    for (int s = 0; s < 4; ++s) {
      const u32x2 a = glu4(o.acc[0][s], o.acc[1][s], cg0, cu0, rs[s], mr[s]), b = glu4(o.acc[2][s], o.acc[3][s], cg1, cu1, rs[s], mr[s]);
      hv[s] = u32x4{a.x, a.y, b.x, b.y};
      if (s == 1) {
        STAMP(19);
        SCHED();
        wait_vm<8>();
        STAMP(20);
        w2_read<2>(c, w2r[2]); w2_read<3>(c, w2r[3]);
        wait_lgkm();
        SCHED();
        w2_issue<6, 2>(p, c); w2_issue<7, 3>(p, c);
        STAMP(21);
        SCHED();
      }
    }
    const f32x4 cgs = cs_rows(c, 4), cus = cs_rows(c, P1<TA>::NT - 1);
    hv[4] = pair_slots(glu4(o.acc[4][0], o.acc[P1<TA>::NT - 1][0], cgs, cus, rs[0], mr[0]), glu4(o.acc[4][1], o.acc[P1<TA>::NT - 1][1], cgs, cus, rs[1], mr[1]));
#pragma unroll
    for (int s = 0; s < 4; ++s) st_sc1(hv[s], rs_ws, a_off(tgp, c.q, c.n, s ^ cx, c.lane), 0);
    st_sc1(hv[4], rs_ws, a_off(tgp, (c.q & 6) + (c.n >> 1), 4, (rho & 1) ^ cx, c.i16 + 16 * (2 * (c.n & 1) + (rho >> 1))), 0);
    STAMP(3);
    SCHED();
    wait_bar<0>();
    if (c.w == 0 && c.lane == 0) flag_st(f2 + (8 + c.q) * 8, c.tag);
    STAMP(5);
    SCHED();
    w2_issue<8, 4>(p, c);
    SCHED();
  }

  relane(c);
  const char* f2c = p.ws + WS_F2 + (c.tg >> 1) * 256;
  const u32 abase = WS_A + (c.tg * 8 + c.w) * 9 * 4096 + c.lane * 16;
  u32x4 af[9][4];
  f32x4 acc[4][4];
#pragma unroll
  for (int v = 0; v < 4; ++v)
#pragma unroll
    for (int t = 0; t < 4; ++t) acc[v][t] = f32x4{0, 0, 0, 0};
  u32x4 kvl[8], zl[2];
  if (TA) {
    poll_tag(p.ws + WS_KVF + (c.q * 16 + c.i16) * 8, c.tag);
    STAMP(6);
    const u32 hd = c.q * 4 + c.n, u = c.lane & 31, ph = c.lane >> 5;
#pragma unroll
    for (int i = 0; i < 8; ++i) kvl[i] = ld_sc1(rs_ws, WS_KVP + ((hd * 16 + 8 * ph) * 2 + c.m) * 512 + u * 16, i * 1024);
#pragma unroll
    for (int s = 0; s < 2; ++s) zl[s] = ld_sc1(rs_ws, WS_ZP + (((hd * 2 + s) * 2 + ph) * 16 + c.i16) * 16, 0);
    SCHED();
  }
  {
    const u32 idx = (c.lane & 3) == 0 ? c.w : ((c.lane & 1) ? 8 + (c.w ^ 1) : 8 + c.w);
    poll_tag(f2c + idx * 8, c.tag);
  }
  STAMP(7);
#pragma unroll
  for (int s = 0; s < 5; ++s) p2_load(rs_ws, abase, s, af[s]);
  SCHED();
  if (TA) {
    float t[8];
#pragma unroll
    for (int j = 0; j < 8; ++j) t[j] = 0.f;
#pragma unroll
    for (int i = 0; i < 8; ++i)
#pragma unroll
      for (int j = 0; j < 4; ++j) { t[2 * j] += asf(kvl[i][j] << 16); t[2 * j + 1] += asf(kvl[i][j] & 0xffff0000u); }
    float r4[4];
#pragma unroll
    for (int j = 0; j < 4; ++j) { u32 a = asu(t[j]), b = asu(t[4 + j]); swap32(a, b); r4[j] = asf(a) + asf(b); }
    const u32 ex = L_SCR + (c.n * 2 + c.m) * 512;
    *(LAS u32x2*)(ex + (c.lane & 31) * 16 + (c.lane >> 5) * 8) = u32x2{pk2(r4[0], r4[1]), pk2(r4[2], r4[3])};
    SCHED();
    lgkm_bar();
    SCHED();
    u32x4 ka[2];
#pragma unroll
    for (int s = 0; s < 2; ++s) {
      const u32x4 kk = lds128(L_SCR + (c.n * 2 + s) * 512 + ((c.lane >> 5) * 16 + c.i16) * 16);
      const bool isz = c.lane & 16;
      ka[s] = u32x4{isz ? zl[s].x : kk.x, isz ? zl[s].y : kk.y, isz ? zl[s].z : kk.z, isz ? zl[s].w : kk.w};
    }
#pragma unroll
    for (int hf = 0; hf < 2; ++hf) {
      f32x16 y;
#pragma unroll
      for (int i = 0; i < 16; ++i) y[i] = 0.f;
      y = mfma32(ka[0], phq[hf][0], y);
      y = mfma32(ka[1], phq[hf][1], y);
      const float dn = ((y[8] + y[9]) + (y[10] + y[11])) + ((y[12] + y[13]) + (y[14] + y[15]));
      u32 a = asu(dn), b = a;
      swap32(a, b);
      const float inv = rcp(asf(a) + asf(b) + 1e-6f);
      const u32x4 ov = {pk2(y[0] * inv, y[1] * inv), pk2(y[2] * inv, y[3] * inv), pk2(y[4] * inv, y[5] * inv), pk2(y[6] * inv, y[7] * inv)};
      st_sc1(ov, rs_ws, a_off(tgp, c.q, 7 + (c.n >> 1), (2 * hf + (rho & 1)) ^ cx, c.i16 + 16 * (2 * (c.n & 1) + (rho >> 1))), 0);
    }
    STAMP(8);
    SCHED();
    p2_mma(acc, w2r[0], af[0]);
    p2_mma(acc, w2r[1], af[1]);
    SCHED();
    wait_bar<0>();
    if (c.w == 0 && c.lane == 0) flag_st(f2 + (16 + c.q) * 8, c.tag);
    STAMP(9);
    SCHED();
    w2_issue<8, 4>(p, c);
    p2_load(rs_ws, abase, 5, af[5]);
    p2_load(rs_ws, abase, 6, af[6]);
    SCHED();
  } else {
    p2_mma(acc, w2r[0], af[0]);
    SCHED();
    p2_load(rs_ws, abase, 5, af[5]);
    SCHED();
    p2_mma(acc, w2r[1], af[1]);
    SCHED();
    p2_load(rs_ws, abase, 6, af[6]);
    SCHED();
  }
  relane(c);
  const u32 tok = c.w * 8 + (c.lane >> 3), c8 = c.lane & 7;
  const size_t oo = (size_t)(c.tg * 64 + tok) * 1024 + c.cg * 128 + c8 * 16;
  const u32x4 xres = *(const GAS u32x4*)(p.x + oo);
  p2_mma(acc, w2r[2], af[2]);
  p2_mma(acc, w2r[3], af[3]);
  SCHED();
  poll_tag(f2c + (16 + c.w) * 8, c.tag);
  STAMP(10);
  p2_load(rs_ws, abase, 7, af[7]);
  p2_load(rs_ws, abase, 8, af[8]);
  SCHED();
  {
    u32x4 wv[4];
    w2_read<0>(c, wv); p2_mma(acc, wv, af[4]);
    w2_read<1>(c, wv); p2_mma(acc, wv, af[5]);
    w2_read<2>(c, wv); p2_mma(acc, wv, af[6]);
    w2_read<3>(c, wv); p2_mma(acc, wv, af[7]);
    w2_read<4>(c, wv); p2_mma(acc, wv, af[8]);
  }
  STAMP(11);
  SCHED();
  lgkm_bar();
  STAMP(23);
  SCHED();
#pragma unroll
  for (int v = 0; v < 4; ++v)
#pragma unroll
    for (int t = 0; t < 4; ++t)
      *(LAS f32x4*)(c.w * 16384 + (16 * t + c.i16) * 256 + (((4 * v + c.g) ^ (c.i16 & 7)) << 4)) = acc[v][t];
  SCHED();
  lgkm_bar();
  STAMP(24);
  SCHED();
  f32x4 r0 = {asf(xres.x << 16), asf(xres.x & 0xffff0000u), asf(xres.y << 16), asf(xres.y & 0xffff0000u)};
  f32x4 r1 = {asf(xres.z << 16), asf(xres.z & 0xffff0000u), asf(xres.w << 16), asf(xres.w & 0xffff0000u)};
#pragma unroll
  for (int ww = 0; ww < 8; ++ww) {
    r0 += lds128f(ww * 16384 + tok * 256 + (((2 * c8) ^ (tok & 7)) << 4));
    r1 += lds128f(ww * 16384 + tok * 256 + (((2 * c8 + 1) ^ (tok & 7)) << 4));
  }
  *(GAS u32x4*)(p.out + oo) = u32x4{pk2(r0[0], r0[1]), pk2(r0[2], r0[3]), pk2(r1[0], r1[1]), pk2(r1[2], r1[3])};
  STAMP(12);
  CSTAMP(31);
}

__global__ __launch_bounds__(512, 2) void fused_kernel(Params p) {
  __shared__ __attribute__((aligned(16))) char smem[163840];
  asm volatile("" ::"v"((LAS char*)smem));
  Ctx c;
  u32* stamps = nullptr;
  STAMP(0);
  c.lane = threadIdx.x & 63;
  c.w = __builtin_amdgcn_readfirstlane(threadIdx.x >> 6);
  c.m = c.w >> 2; c.n = c.w & 3; c.g = c.lane >> 4; c.i16 = c.lane & 15;
  const u32 b = blockIdx.x, xc = b & 7, r = b >> 3;
  c.ti = (xc & 1) * 8 + (r & 7);
  const u32 cjl = r >> 3;
  c.q = (xc >> 1) * 2 + (cjl & 1);
  c.tg = xc * 4 + (r & 3); c.cg = r >> 2;
  const u32 off = STAGGER ? ((2 * cjl + (r & 7)) & 7) : 0;
  {
    u64 pc; u32 len;
    asm volatile("s_getpc_b64 %0\n.Lfk_pc:\n\ts_mov_b32 %1, .Lfk_end-.Lfk_pc" : "=s"(pc), "=s"(len));
    u32 o = threadIdx.x * 128;
    o = o < len ? o : 0;
    asm volatile("s_mov_b32 m0, %2\n\ts_nop 0\n\tglobal_load_lds_dword %0, %1" ::"v"(o), "s"(pc), "s"(L_JUNK + c.w * 256) : "memory");
  }
  {
    const u32 t = threadIdx.x, w = c.w;
    const char* a;
    if (TLBW == 1) {
      a = t < 168 ? p.ws + t * 65536u + 7168 : (t < 196 ? p.wd + (t - 168) * 65536u : (t < 204 ? p.wo + (t - 196) * 65536u : p.out + c.tg * 65536u));
      asm volatile("s_mov_b32 m0, %1\n\ts_nop 0\n\tglobal_load_lds_dword %0, off" ::"v"(a), "s"(L_JUNK + w * 256) : "memory");
    } else {
      a = t < 448 ? p.wd + t * 4096u : p.wo + (t - 448) * 8192u;
      asm volatile("s_mov_b32 m0, %1\n\ts_nop 0\n\tglobal_load_lds_dword %0, off" ::"v"(a), "s"(L_JUNK + w * 256) : "memory");
      a = t < 72 ? p.ws + WS_A + c.tg * 294912u + t * 4096u : (t < 216 ? p.ws + WS_A + c.ti * 589824u + (t - 72) * 4096u : (t < 232 ? p.out + c.tg * 65536u + (t - 216) * 4096u : (t < 296 ? p.wo + (t - 232) * 8192u + 4096u : (t < 440 ? p.ws + (t - 296) * 4096u + 1536 : p.ws + 1536))));
      asm volatile("s_mov_b32 m0, %1\n\ts_nop 0\n\tglobal_load_lds_dword %0, off" ::"v"(a), "s"(L_JUNK + w * 256) : "memory");
    }
  }
  if (cjl < 2) {
    relane(c);
    if (c.w < 2) glds16s(p.wphi + c.q * 2048 + c.w * 1024, c.lane * 16, L_WPH + c.w * 1024);
    block_body<true>(p, c, off, stamps);
  } else {
    block_body<false>(p, c, off, stamps);
  }
  relane(c);
  if (b == 0 && c.w == 0) {
    const char* f = p.ws + WS_F2 + (c.lane >> 2) * 256 + (c.lane & 3) * 32;
    poll_tag(f, c.tag); poll_tag(f + 8, c.tag); poll_tag(f + 16, c.tag); poll_tag(f + 24, c.tag);
    if (c.lane == 0) flag_st(p.ws + WS_E, c.tag);
  }
  asm volatile(".Lfk_end:");
}

extern "C" void kernel_launch(void* const* d_in, const int* in_sizes, int n_in, void* d_out, int out_size, void* d_ws, size_t ws_size,
                              hipStream_t stream) {
  Params p;
  p.x = (const char*)d_in[0]; p.wqkv = (const char*)d_in[1]; p.wphi = (const char*)d_in[2]; p.wo = (const char*)d_in[3];
  p.wg = (const char*)d_in[5]; p.wu = (const char*)d_in[6]; p.wd = (const char*)d_in[7];
  p.out = (char*)d_out; p.ws = (char*)d_ws;
  fused_kernel<<<256, 512, 0, stream>>>(p);
}
```

```cpp
#define DIAG_A 0
#define DIAG_B 13
#define DIAG_K 8
#include <hip/hip_runtime.h>
#include <stdint.h>
#include <stdio.h>
#pragma clang diagnostic ignored "-Wint-to-pointer-cast"

typedef unsigned u32;
typedef unsigned long long u64;
typedef __attribute__((ext_vector_type(4))) unsigned u32x4;
typedef __attribute__((ext_vector_type(2))) unsigned u32x2;
typedef __attribute__((ext_vector_type(2))) float f32x2;
typedef __attribute__((ext_vector_type(4))) float f32x4;
typedef __attribute__((ext_vector_type(16))) float f32x16;
typedef __attribute__((ext_vector_type(8))) __bf16 bf16x8;
typedef __attribute__((ext_vector_type(2))) __bf16 bf16x2;
typedef __attribute__((ext_vector_type(4))) short s16x4;
#define LAS __attribute__((address_space(3)))
#define GAS __attribute__((address_space(1)))
#define CAS __attribute__((address_space(4)))
#define DI __device__ __forceinline__

#define ABL_NOSWZ 0
#define ABL_NOCS 0
#define ABL_NOSTAT 0
#define ABL_NOLD 0
#define ABL_NORD 0
#define STATPOS 0
#define STAGGER 1
#define CODE_PF 1

constexpr u32 WS_E = 0;
constexpr u32 WS_KVF = 256;
constexpr u32 WS_F2 = 2048;
constexpr u32 WS_KVP = 8192;
constexpr u32 WS_ZP = WS_KVP + 524288;
constexpr u32 WS_A = 1u << 20;
constexpr u32 WS_BYTES = WS_A + 32u * 72u * 4096u;
constexpr u32 WS_DBG = 16u << 20;

constexpr u32 L_X = 0;
constexpr u32 L_W = 49152;
constexpr u32 L_M = 147456;
constexpr u32 L_ST = L_M;
constexpr u32 L_CSP = L_M + 1024;
constexpr u32 L_WPH = L_M + 4096;
constexpr u32 L_JUNK = L_M + 6144;
constexpr u32 L_SCR = L_X + 16384;

constexpr float LOG2E = 1.4426950408889634f;

struct Params {
  const char* x; const char* wqkv; const char* wphi; const char* wo;
  const char* wg; const char* wu; const char* wd;
  char* out; char* ws;
};

#define SCHED() __builtin_amdgcn_sched_barrier(0)
template <int N> DI void wait_vm() { asm volatile("s_waitcnt vmcnt(%0)" ::"n"(N) : "memory"); }
template <int N> DI void wait_bar() { asm volatile("s_waitcnt vmcnt(%0) lgkmcnt(0)\n\ts_barrier" ::"n"(N) : "memory"); }
#define PRO_BAR() asm volatile("s_barrier" ::: "memory")
DI void lgkm0() { __builtin_amdgcn_s_waitcnt(0xC07F); }
DI void lgkm_bar() { asm volatile("s_waitcnt lgkmcnt(0)\n\ts_barrier" ::: "memory"); }
DI void wait_lgkm() { asm volatile("s_waitcnt lgkmcnt(0)" ::: "memory"); }

DI void glds16(const char* g, u32 lds) {
  asm volatile("s_mov_b32 m0, %1\n\ts_nop 0\n\tglobal_load_lds_dwordx4 %0, off" ::"v"(g), "s"(lds) : "memory");
}
DI void glds16o128(const char* g, u32 lds) {
  asm volatile("s_mov_b32 m0, %1\n\ts_nop 0\n\tglobal_load_lds_dwordx4 %0, off offset:128" ::"v"(g), "s"(lds - 128) : "memory");
}
DI void glds16s(const char* sbase, u32 voff, u32 lds) {
  asm volatile("s_mov_b32 m0, %2\n\ts_nop 0\n\tglobal_load_lds_dwordx4 %0, %1" ::"v"(voff), "s"(sbase), "s"(lds) : "memory");
}
DI u32x4 lds128(u32 a) { return *(const LAS u32x4*)(a); }
DI f32x4 lds128f(u32 a) { return *(const LAS f32x4*)(a); }
DI u32x2 ldstr(u32 a) { return __builtin_bit_cast(u32x2, __builtin_amdgcn_ds_read_tr16_b64_v4i16((LAS s16x4*)(a))); }
DI u32x4 ldstr2(u32 a0, u32 a1) { u32x2 lo = ldstr(a0), hi = ldstr(a1); return u32x4{lo.x, lo.y, hi.x, hi.y}; }
DI f32x4 mfma16(u32x4 a, u32x4 b, f32x4 c) {
  return __builtin_amdgcn_mfma_f32_16x16x32_bf16(__builtin_bit_cast(bf16x8, a), __builtin_bit_cast(bf16x8, b), c, 0, 0, 0);
}
DI f32x16 mfma32(u32x4 a, u32x4 b, f32x16 c) {
  return __builtin_amdgcn_mfma_f32_32x32x16_bf16(__builtin_bit_cast(bf16x8, a), __builtin_bit_cast(bf16x8, b), c, 0, 0, 0);
}
DI u32 pk2(float a, float b) { bf16x2 v = {(__bf16)a, (__bf16)b}; return __builtin_bit_cast(u32, v); }
DI float asf(u32 x) { return __builtin_bit_cast(float, x); }
DI u32 asu(float x) { return __builtin_bit_cast(u32, x); }
DI float fmax2(float a, float b) { return __builtin_elementwise_maximum(a, b); }
DI void swap16(u32& a, u32& b) { auto r = __builtin_amdgcn_permlane16_swap(a, b, false, false); a = r[0]; b = r[1]; }
DI void swap32(u32& a, u32& b) { auto r = __builtin_amdgcn_permlane32_swap(a, b, false, false); a = r[0]; b = r[1]; }
DI float ex2(float x) { return __builtin_amdgcn_exp2f(x); }
DI float rcp(float x) { return __builtin_amdgcn_rcpf(x); }

typedef __amdgpu_buffer_rsrc_t rsrc_t;
DI u32x4 ld_sc1(rsrc_t rs, u32 voff, u32 soff) { return __builtin_amdgcn_raw_buffer_load_b128(rs, voff, soff, 16); }
DI void st_sc1(u32x4 v, rsrc_t rs, u32 voff, u32 soff) { __builtin_amdgcn_raw_buffer_store_b128(v, rs, voff, soff, 16); }
DI u64 flag_ld(const char* p) { return __hip_atomic_load((GAS u64*)p, __ATOMIC_RELAXED, __HIP_MEMORY_SCOPE_AGENT); }
DI void flag_st(char* p, u64 v) { __hip_atomic_store((GAS u64*)p, v, __ATOMIC_RELAXED, __HIP_MEMORY_SCOPE_AGENT); }
DI void poll_tag(const char* p, u64 tag) {
#pragma unroll 1
  for (u32 spins = 0; spins < (1u << 20); ++spins) {
    u64 v = flag_ld(p);
    if (__all(v == tag)) break;
    __builtin_amdgcn_s_sleep(1);
  }
  __builtin_amdgcn_fence(__ATOMIC_ACQUIRE, "wavefront");
}

#define CSTAMP(i) do {} while (0)
#define STAMP(i) do { if ((i) == DIAG_A || (i) == DIAG_B) { SCHED(); u64 t_; asm volatile("s_memrealtime %0\n\ts_waitcnt lgkmcnt(0)" : "=s"(t_)::"memory"); stamps[(i) == DIAG_A ? 0 : 1] = (u32)t_; SCHED(); } } while (0)

struct Ctx {
  u32 w, m, n, lane, g, i16;
  u32 ti, q, cg, tg;
  u64 tag;
};

DI void relane(Ctx& c) {
  u32 l = __builtin_amdgcn_mbcnt_hi(~0u, __builtin_amdgcn_mbcnt_lo(~0u, 0u));
  asm volatile("" : "+v"(l));
  c.lane = l; c.g = l >> 4; c.i16 = l & 15;
}
DI u32 w2_area_piece(u32 area, u32 piece, u32 w) {
  if (area < 4) return piece < 3 ? L_W + area * 24576 + w * 3072 + piece * 1024
                                 : L_X + (area < 2 ? 32768 : 0) + (area & 1) * 8192 + w * 1024;
  return piece < 2 ? L_SCR + w * 2048 + piece * 1024 : L_M + w * 2048 + (piece - 2) * 1024;
}
template <int STEP, int AREA> DI void w2_issue(const Params& p, const Ctx& c, int g0 = 0, int g1 = 4) {
  const u32 j = c.lane >> 3;
  const u32 rowl = STEP < 4 ? 64 * (j >> 2) + (j & 3) : (STEP < 7 ? j : (j & 3) + 8 * (j >> 2));
  const u32 P0 = 8 + 5 * (c.w >> 1) + 2 * (c.w & 1), PS = 8 + 5 * (c.w >> 1) + 4;
#pragma unroll
  for (int g = g0; g < g1; ++g) {
    const u32 sw = (((j >> 1) & 1) << 1) | ((g & 1) << 2);
    const u32 voff = rowl * 1024 + (((c.lane & 7) ^ sw) << 4);
    u32 rows;
    if (STEP < 4) rows = 64 * P0 + 16 * STEP + 4 * g;
    else if (STEP == 4) rows = 64 * PS + 32 * (c.w & 1) + 8 * g;
    else if (STEP < 7) rows = 64 * c.w + 32 * (STEP - 5) + 8 * g;
    else rows = 64 * c.w + 16 * (2 * (STEP - 7) + (g >> 1)) + 4 * (g & 1);
    const char* base = (STEP < 7 ? p.wd : p.wo) + (size_t)(rows * 1024 + c.cg * 128);
    glds16s(base, voff, w2_area_piece(AREA, g, c.w));
  }
}
template <int AREA> DI void w2_read(const Ctx& c, u32x4 (&f)[4]) {
  const u32 qq = c.i16 >> 2, pp = c.i16 & 3;
  u32 pb;
  if (AREA < 4) pb = c.g < 3 ? L_W + AREA * 24576 + c.w * 3072 + c.g * 1024 : L_X + (AREA < 2 ? 32768 : 0) + (AREA & 1) * 8192 + c.w * 1024;
  else pb = c.g < 2 ? L_SCR + c.w * 2048 + c.g * 1024 : L_M + c.w * 2048 + (c.g - 2) * 1024;
  const u32 sw = (((qq >> 1) & 1) << 1) | ((c.g & 1) << 2);
  const u32 a = pb + qq * 128 + (((pp >> 1) ^ sw) << 4) + 8 * (pp & 1);
#pragma unroll
  for (int v = 0; v < 4; ++v) f[v] = ldstr2((a ^ (v * 32)), (a ^ (v * 32)) + 512);
}

template <bool TA> struct P1 {
  static constexpr int NT = TA ? 5 : 6;
  f32x4 acc[NT][4];
  f32x4 acs[NT];
  float s1, s2;
};

template <bool TA> DI void main_loop(const Params& p, Ctx& c, u32 off, u32 cx, P1<TA>& o, u32* stamps) {
  constexpr int NT = P1<TA>::NT;
  const u32 lane = c.lane, w = c.w;
  const char* xs = p.x + (size_t)c.ti * 131072;
  u32 xv[2];
#pragma unroll
  for (int pc = 0; pc < 2; ++pc) xv[pc] = (16 * w + 8 * pc + (lane >> 3)) * 1024 + (((lane & 7) ^ (ABL_NOSWZ ? 0 : (4 * pc + (lane >> 4)))) << 4);
  const char* wsrc[3];
  {
    const u32 r = (lane >> 3) & 3, hi = lane >> 5;
    const u32 sw = (((r >> 1) & 1) << 1) | (((w >> 1) & 1) << 2);
    const u32 ch = (((lane & 7) ^ (ABL_NOSWZ ? 0 : sw)) << 4);
    const u32 kr = 4 * w + r;
    if (TA) {
      wsrc[0] = p.wqkv + (kr * 3072 + hi * 1024 + c.q * 128 + ch);
      wsrc[1] = (hi ? p.wu : p.wg) + (kr * 3584 + c.q * 128 + ch);
      wsrc[2] = p.wqkv + (kr * 3072 + 2048 + c.q * 128 + ch);
    } else {
      const u32 P0 = 8 + 5 * (c.q >> 1) + 2 * (c.q & 1), PS = 8 + 5 * (c.q >> 1) + 4;
      const char* b = (hi ? p.wu : p.wg) + (kr * 3584 + ch);
      wsrc[0] = b + P0 * 128; wsrc[1] = b + (P0 + 1) * 128; wsrc[2] = b + PS * 128;
    }
  }
  auto issue_x = [&](int c8, int pc) {
    const u32 kc = (c8 + off) & 7;
    glds16s(xs + kc * 128, xv[pc], L_X + (c8 % 3) * 16384 + (16 * w + 8 * pc) * 128);
  };
  auto issue_w1 = [&](int s, int pc) {
    const u32 k0 = (((s >> 1) + off) & 7) * 64 + (s & 1) * 32;
    const u32 dst = L_W + (s & 3) * 24576 + w * 3072;
    if (TA) {
      if (pc == 0) glds16(wsrc[0] + (size_t)(k0 * 3072), dst);
      else if (pc == 1) glds16(wsrc[1] + (size_t)(k0 * 3584), dst + 1024);
      else if (lane < 32) glds16(wsrc[2] + (size_t)(k0 * 3072), dst + 2048);
    } else {
      if (pc == 0) glds16(wsrc[0] + (size_t)(k0 * 3584), dst);
      else if (pc == 1) glds16o128(wsrc[0] + (size_t)(k0 * 3584), dst + 1024);
      else glds16(wsrc[2] + (size_t)(k0 * 3584), dst + 2048);
    }
  };
  auto issue_w = [&](int s) { issue_w1(s, 0); issue_w1(s, 1); issue_w1(s, 2); };
  STAMP(13);
  u64 ep;
  asm volatile("s_load_dwordx2 %0, %1, 0x0" : "=s"(ep) : "s"(p.ws + WS_E) : "memory");
  issue_x(0, 0); issue_x(0, 1); issue_w(0);
  STAMP(25);
  PRO_BAR();
  issue_x(1, 0); issue_w(1);
  u32 xa[2][4];
#pragma unroll
  for (int s = 0; s < 4; ++s) {
    const u32 row = 64 * c.m + 16 * (s ^ cx) + c.i16;
    xa[0][s] = L_X + row * 128 + ((c.g ^ (c.i16 >> 1)) << 4);
    xa[1][s] = xa[0][s] ^ 64;
  }
  u32 wa;
  {
    const u32 qq = c.i16 >> 2, pp = c.i16 & 3;
    const u32 sw = (((qq >> 1) & 1) << 1) | ((c.g & 1) << 2);
    wa = L_W + 2 * c.g * 3072 + qq * 128 + (((2 * c.n + (pp >> 1)) ^ sw) << 4) + 8 * (pp & 1);
  }
  u32 wb = wa + 49152;
  asm volatile("" : "+v"(wa), "+v"(wb));
  u32x4 xf[2][4], wf[2][NT];
  u32x2 wh[2][NT][2];
  auto read1 = [&](int s, int i) {
    const int b = s & 1;
    if (i >= 2 && i < 6) { xf[b][i - 2] = lds128(xa[s & 1][i - 2] + ((s >> 1) % 3) * 16384); return; }
    const int k = i < 2 ? i : i - 4, l = k >> 1, t = k & 1;
    if (l >= NT) return;
    wh[b][l][t] = ldstr(((s & 2) ? wb : wa) + (s & 1) * 24576 + l * 512 + t * 3072);
    if (t) wf[b][l] = u32x4{wh[b][l][0].x, wh[b][l][0].y, wh[b][l][1].x, wh[b][l][1].y};
  };
  auto mm = [&](int b, int i) {
    if (i < 16) { o.acc[i >> 2][i & 3] = mfma16(wf[b][i >> 2], xf[b][i & 3], o.acc[i >> 2][i & 3]); return; }
    if (TA) o.acc[4][i & 3] = mfma16(xf[b][i & 3], wf[b][4], o.acc[4][i & 3]);
    else { const int l = 4 + ((i >> 1) & 1) * (NT - 5), t = i & 1; o.acc[l][t] = mfma16(wf[b][l], xf[b][t], o.acc[l][t]); }
  };
  auto stat = [&](int b, int u) {
    if (ABL_NOSTAT) return;
    const u32 d = xf[b][0][u >> 1];
    const float v = asf((u & 1) ? (d & 0xffff0000u) : (d << 16));
    o.s1 += v; o.s2 = __builtin_fmaf(v, v, o.s2);
    asm volatile("" : "+v"(o.s1), "+v"(o.s2));
  };
  auto issue1 = [&](int s, int pc) {
    if (ABL_NOLD) return;
    if (s < 12) { if (pc == 0) { if (s <= 10) issue_x((s + 5) >> 1, (s + 5) & 1); } else issue_w1(s + 4, pc - 1); }
    else if (s == 12) w2_issue<0, 0>(p, c, pc, pc + 1);
    else if (s == 13) w2_issue<1, 1>(p, c, pc, pc + 1);
    else if (s == 14) w2_issue<2, 2>(p, c, pc, pc + 1);
    else w2_issue<3, 3>(p, c, pc, pc + 1);
  };
#pragma unroll
  for (int l = 0; l < NT; ++l) {
    o.acs[l] = f32x4{0, 0, 0, 0};
#pragma unroll
    for (int t = 0; t < 4; ++t) o.acc[l][t] = f32x4{0, 0, 0, 0};
  }
  o.s1 = 0.f; o.s2 = 0.f;
  const u32x4 ones = {0x3F803F80u, 0x3F803F80u, 0x3F803F80u, 0x3F803F80u};
  STAMP(14);
  SCHED();
  wait_bar<4>();
  asm volatile("" : "+s"(ep));
  ep += 0x9E3779B97F4A7C15ull;
  ep = (ep ^ (ep >> 30)) * 0xBF58476D1CE4E5B9ull;
  ep = (ep ^ (ep >> 27)) * 0x94D049BB133111EBull;
  c.tag = ep ^ (ep >> 31);
  STAMP(1);
  SCHED();
#pragma unroll
  for (int i = 0; i < 16; ++i) read1(0, i);
  SCHED();
#pragma unroll
  for (int s = 0; s < 16; ++s) {
    const int b = s & 1;
    if (s == 13) relane(c);
#pragma unroll
    for (int i = 0; i < 10; ++i) {
      mm(b, i);
      if (s > 0 && (i & 1) == 0 && i < 8) issue1(s - 1, i >> 1);
      if (s == 0 && i < 8) issue1(i < 4 ? -2 : -1, i & 3);
      if (STATPOS == 0) { if (i & 1) stat(b, i >> 1); if (i == 8) stat(b, 5); }
      if (STATPOS == 1) { if (i == 1 || i == 5 || i == 9) stat(b, i >> 2); }
      if (STATPOS == 2) { if (i == 8 || i == 9) stat(b, i - 8); }
      SCHED();
    }
    lgkm0();
    if (s == 15) wait_bar<63>();
    else if (s == 12 || s == 13) wait_bar<7>();
    else wait_bar<8>();
    if (s == 0) STAMP(27);
    if (s == 3) { STAMP(28); CSTAMP(29); }
    SCHED();
#pragma unroll
    for (int i = 0; i < 10; ++i) {
      mm(b, 10 + i);
      if (s < 15 && i < 8 && !(ABL_NORD && s > 0)) { read1(s + 1, 2 * i); read1(s + 1, 2 * i + 1); }
      if (STATPOS == 0) { if (i >= 8) stat(b, i - 2); }
      if (STATPOS == 1) { if (i & 1) stat(b, 3 + (i >> 1)); }
      if (STATPOS == 2) { if (i >= 4) stat(b, i - 2); }
      SCHED();
    }
    if (!ABL_NOCS && c.m == (u32)(s & 1)) {
#pragma unroll
      for (int l = 0; l < NT; ++l) {
        if (TA && l == 4) o.acs[l] = mfma16(ones, wf[b][l], o.acs[l]);
        else o.acs[l] = mfma16(wf[b][l], ones, o.acs[l]);
      }
    }
    SCHED();
  }
#pragma unroll
  for (int pc = 0; pc < 4; ++pc) issue1(15, pc);
  STAMP(26);
}

template <bool TA> DI void stats_exchange(const Ctx& c, u32 cx, P1<TA>& o, float (&rs)[4], float (&mr)[4]) {
  constexpr int NT = P1<TA>::NT;
  u32 a = asu(o.s1), b = asu(o.s2);
  swap32(a, b);
  float r = asf(a) + asf(b);
  a = asu(r); b = a;
  swap16(a, b);
  r = asf(a) + asf(b);
  a = asu(r); b = a;
  swap32(a, b);
  const float mean = asf(a) * (1.f / 512.f);
  const float var = __builtin_fmaxf(__builtin_fmaf(-mean, mean, asf(b) * (1.f / 512.f)), 0.f);
  const float rstd = __builtin_amdgcn_rsqf(var + 1e-5f);
  if (c.lane < 16) *(LAS f32x2*)(L_ST + (64 * c.m + 16 * cx + c.i16) * 8) = f32x2{rstd, mean * rstd};
  const u32 cb = L_CSP + ((c.m * 4 + c.n) * 6) * 64;
#pragma unroll
  for (int l = 0; l < NT; ++l) {
    if (TA && l == 4) { if (c.lane < 16) *(LAS float*)(cb + l * 64 + c.i16 * 4) = o.acs[l][0]; }
    else if (c.i16 == 0) *(LAS f32x4*)(cb + l * 64 + c.g * 16) = o.acs[l];
  }
  SCHED();
  lgkm_bar();
  SCHED();
#pragma unroll
  for (int s = 0; s < 4; ++s) {
    const f32x2 v = *(const LAS f32x2*)(L_ST + (64 * c.m + 16 * (s ^ cx) + c.i16) * 8);
    rs[s] = v.x; mr[s] = v.y;
  }
}
DI f32x4 cs_rows(const Ctx& c, int l) {
  return lds128f(L_CSP + (c.n * 6 + l) * 64 + c.g * 16) + lds128f(L_CSP + ((4 + c.n) * 6 + l) * 64 + c.g * 16);
}
DI u32x2 glu4(f32x4 ag, f32x4 au, f32x4 cg, f32x4 cu, float rs, float mr) {
  const float nrs = -LOG2E * rs, lmr = LOG2E * mr;
  float h[4];
#pragma unroll
  for (int r = 0; r < 4; ++r) {
    const float e = ex2(__builtin_fmaf(ag[r], nrs, lmr * cg[r]));
    const float u = __builtin_fmaf(au[r], rs, -(mr * cu[r]));
    h[r] = u * rcp(1.f + e);
  }
  return u32x2{pk2(h[0], h[1]), pk2(h[2], h[3])};
}
DI u32x2 ln4(f32x4 a, f32x4 cs, float rs, float mr) {
  float y[4];
#pragma unroll
  for (int r = 0; r < 4; ++r) y[r] = __builtin_fmaf(a[r], rs, -(mr * cs[r]));
  return u32x2{pk2(y[0], y[1]), pk2(y[2], y[3])};
}
DI u32x4 pair_slots(u32x2 x, u32x2 y) {
  u32 x0 = x.x, x1 = x.y, y0 = y.x, y1 = y.y;
  swap16(x0, y0); swap16(x1, y1);
  return u32x4{x0, x1, y0, y1};
}
DI void features(u32x4 aw, u32x4 b, u32x4 (&ph)[2]) {
  f32x16 z;
#pragma unroll
  for (int i = 0; i < 16; ++i) z[i] = 0.f;
  const f32x16 y = mfma32(aw, b, z);
  float mp = fmax2(fmax2(fmax2(y[0], y[1]), y[2]), fmax2(fmax2(y[3], y[4]), y[5]));
  mp = fmax2(fmax2(mp, y[6]), y[7]);
  float mn = fmax2(fmax2(fmax2(y[8], y[9]), y[10]), fmax2(fmax2(y[11], y[12]), y[13]));
  mn = fmax2(fmax2(mn, y[14]), y[15]);
  u32 a = asu(mp), bb = asu(mn);
  swap32(a, bb);
  a = asu(fmax2(asf(a), asf(bb)) * -LOG2E); bb = a;
  swap32(a, bb);
  float e[16];
#pragma unroll
  for (int i = 0; i < 8; ++i) { e[i] = ex2(__builtin_fmaf(y[i], LOG2E, asf(a))); e[8 + i] = ex2(__builtin_fmaf(y[8 + i], LOG2E, asf(bb))); }
  const float sp = ((e[0] + e[1]) + (e[2] + e[3])) + ((e[4] + e[5]) + (e[6] + e[7]));
  const float sn = ((e[8] + e[9]) + (e[10] + e[11])) + ((e[12] + e[13]) + (e[14] + e[15]));
  a = asu(sp); bb = asu(sn);
  swap32(a, bb);
  a = asu(rcp(asf(a) + asf(bb))); bb = a;
  swap32(a, bb);
  const float ip = asf(a), in = asf(bb);
#pragma unroll
  for (int i = 0; i < 4; ++i) { ph[0][i] = pk2(e[2 * i] * ip, e[2 * i + 1] * ip); ph[1][i] = pk2(e[8 + 2 * i] * in, e[9 + 2 * i] * in); }
}

DI u32 a_off(u32 tg, u32 wsl, u32 step, u32 tb, u32 lanep) { return WS_A + (((tg * 8 + wsl) * 9 + step) * 4 + tb) * 1024 + lanep * 16; }

DI void p2_load(rsrc_t rs, u32 vbase, int step, u32x4 (&f)[4]) {
#pragma unroll
  for (int t = 0; t < 4; ++t) f[t] = ld_sc1(rs, vbase + t * 1024, step * 4096);
}
DI void p2_mma(f32x4 (&acc)[4][4], const u32x4 (&wv)[4], const u32x4 (&af)[4]) {
#pragma unroll
  for (int t = 0; t < 4; ++t)
#pragma unroll
    for (int v = 0; v < 4; ++v) acc[v][t] = mfma16(wv[v], af[t], acc[v][t]);
}

template <bool TA> DI void block_body(const Params& p, Ctx& c, u32 off, u32* stamps) {
  const u32 cx = TA ? c.n : (c.n ^ ((c.q & 1) << 1));
  const rsrc_t rs_ws = __builtin_amdgcn_make_buffer_rsrc(p.ws, 0, WS_BYTES, 0x00020000);
  P1<TA> o;
  relane(c);
  main_loop<TA>(p, c, off, cx, o, stamps);
  STAMP(2);
  CSTAMP(30);
  relane(c);
  float rs[4], mr[4];
  stats_exchange<TA>(c, cx, o, rs, mr);
  u32x4 w2r[4][4];
  const u32 tgp = c.ti * 2 + c.m;
  const u32 rho = c.g;
  char* f2 = p.ws + WS_F2 + c.ti * 256;
  STAMP(16);
  SCHED();
  wait_vm<8>();
  STAMP(17);
  w2_read<0>(c, w2r[0]); w2_read<1>(c, w2r[1]);
  wait_lgkm();
  SCHED();
  w2_issue<4, 0>(p, c); w2_issue<5, 1>(p, c);
  STAMP(18);
  SCHED();
  u32x4 phq[2][2];
  if (TA) {
    u32x4 aw;
    {
      const u32 qq = c.i16 >> 2, pp = c.i16 & 3, h = c.lane >> 5;
      const u32 a = L_WPH + c.n * 512 + (8 * h + qq) * 32 + pp * 8;
      aw = ldstr2(a, a + 128);
      const u32 sg = (c.lane & 16) ? 0x80008000u : 0u;
      aw.x ^= sg; aw.y ^= sg; aw.z ^= sg; aw.w ^= sg;
    }
    const f32x4 csk = cs_rows(c, 1);
    const float csv = *(const LAS float*)(L_CSP + (c.n * 6 + 4) * 64 + c.i16 * 4) + *(const LAS float*)(L_CSP + ((4 + c.n) * 6 + 4) * 64 + c.i16 * 4);
    f32x16 kv;
#pragma unroll
    for (int i = 0; i < 16; ++i) kv[i] = 0.f;
    const u32 img = L_SCR + c.w * 2048;
    const u32 cone = c.i16 == 0 ? 0x3F803F80u : 0u;
#pragma unroll
    for (int hf = 0; hf < 2; ++hf) {
      const u32x4 kb = pair_slots(ln4(o.acc[1][2 * hf], csk, rs[2 * hf], mr[2 * hf]), ln4(o.acc[1][2 * hf + 1], csk, rs[2 * hf + 1], mr[2 * hf + 1]));
      u32x4 ph[2];
      features(aw, kb, ph);
      {
        const u32 t = c.lane & 31, h = c.lane >> 5, x = (t >> 1) & 7;
        if (hf) wait_lgkm();
#pragma unroll
        for (int G = 0; G < 4; ++G)
          *(LAS u32x2*)(img + t * 64 + (((2 * G + h) ^ x) << 3)) = u32x2{ph[G >> 1][2 * (G & 1)], ph[G >> 1][2 * (G & 1) + 1]};
      }
      wait_lgkm();
#pragma unroll
      for (int ks = 0; ks < 2; ++ks) {
        const int s = 2 * hf + ks;
        const u32 qq = c.i16 >> 2, pp = c.i16 & 3, blk = (c.lane >> 4) & 1, h = c.lane >> 5;
        const u32 r0 = 16 * ks + 8 * h + qq, r1 = r0 + 4;
        const u32x4 pa = ldstr2(img + r0 * 64 + (((4 * blk + pp) ^ ((r0 >> 1) & 7)) << 3), img + r1 * 64 + (((4 * blk + pp) ^ ((r1 >> 1) & 7)) << 3));
        const u32 sa = L_ST + (64 * c.m + 16 * (s ^ cx) + 4 * c.g) * 8;
        const f32x4 st0 = lds128f(sa), st1 = lds128f(sa + 16);
        const float v0 = __builtin_fmaf(o.acc[4][s][0], st0[0], -(st0[1] * csv)), v1 = __builtin_fmaf(o.acc[4][s][1], st0[2], -(st0[3] * csv));
        const float v2 = __builtin_fmaf(o.acc[4][s][2], st1[0], -(st1[1] * csv)), v3 = __builtin_fmaf(o.acc[4][s][3], st1[2], -(st1[3] * csv));
        const u32x4 vb = pair_slots(u32x2{pk2(v0, v1), pk2(v2, v3)}, u32x2{cone, cone});
        kv = mfma32(pa, vb, kv);
      }
      if (hf == 0) {
        STAMP(19);
        SCHED();
        wait_vm<8>();
        STAMP(20);
        w2_read<2>(c, w2r[2]); w2_read<3>(c, w2r[3]);
        wait_lgkm();
        SCHED();
        w2_issue<6, 2>(p, c); w2_issue<7, 3>(p, c);
        STAMP(21);
        SCHED();
      }
    }
    STAMP(22);
    {
      f32x4 s0, s1, k0, k1;
#pragma unroll
      for (int i = 0; i < 4; ++i) {
        s0[i] = c.m ? kv[i] : kv[8 + i]; s1[i] = c.m ? kv[4 + i] : kv[12 + i];
        k0[i] = c.m ? kv[8 + i] : kv[i]; k1[i] = c.m ? kv[12 + i] : kv[4 + i];
      }
      wait_lgkm();
      *(LAS f32x4*)(img + c.lane * 16) = s0;
      *(LAS f32x4*)(img + 1024 + c.lane * 16) = s1;
      SCHED();
      lgkm_bar();
      SCHED();
      const u32 pim = L_SCR + (c.w ^ 4) * 2048;
      k0 += lds128f(pim + c.lane * 16);
      k1 += lds128f(pim + 1024 + c.lane * 16);
      const u32x4 v = {pk2(k0[0], k0[1]), pk2(k0[2], k0[3]), pk2(k1[0], k1[1]), pk2(k1[2], k1[3])};
      const u32 d = c.lane & 31, h = c.lane >> 5, hd = c.q * 4 + c.n;
      const u32 vo = d < 16 ? WS_KVP + ((hd * 16 + c.ti) * 2 + c.m) * 512 + (h * 16 + d) * 16
                            : WS_ZP + (((hd * 2 + c.m) * 2 + h) * 16 + c.ti) * 16;
      if (d <= 16) st_sc1(v, rs_ws, vo, 0);
    }
    STAMP(3);
    SCHED();
    u32x4 hv[2];
    {
      const f32x4 csg = cs_rows(c, 2), csu = cs_rows(c, 3);
#pragma unroll
      for (int hf = 0; hf < 2; ++hf)
        hv[hf] = pair_slots(glu4(o.acc[2][2 * hf], o.acc[3][2 * hf], csg, csu, rs[2 * hf], mr[2 * hf]),
                            glu4(o.acc[2][2 * hf + 1], o.acc[3][2 * hf + 1], csg, csu, rs[2 * hf + 1], mr[2 * hf + 1]));
    }
    const f32x4 csq = cs_rows(c, 0);
    SCHED();
    wait_bar<0>();
    if (c.w == 0 && c.lane == 0) flag_st(p.ws + WS_KVF + (c.q * 16 + c.ti) * 8, c.tag);
    STAMP(4);
    SCHED();
#pragma unroll
    for (int hf = 0; hf < 2; ++hf)
      st_sc1(hv[hf], rs_ws, a_off(tgp, c.q, 5 + (c.n >> 1), (2 * hf + (rho & 1)) ^ cx, c.i16 + 16 * (2 * (c.n & 1) + (rho >> 1))), 0);
    SCHED();
#pragma unroll
    for (int hf = 0; hf < 2; ++hf) {
      const u32x4 qb = pair_slots(ln4(o.acc[0][2 * hf], csq, rs[2 * hf], mr[2 * hf]), ln4(o.acc[0][2 * hf + 1], csq, rs[2 * hf + 1], mr[2 * hf + 1]));
      features(aw, qb, phq[hf]);
    }
    SCHED();
    wait_bar<0>();
    if (c.w == 0 && c.lane == 0) flag_st(f2 + c.q * 8, c.tag);
    STAMP(5);
  } else {
    const f32x4 cg0 = cs_rows(c, 0), cu0 = cs_rows(c, 1), cg1 = cs_rows(c, 2), cu1 = cs_rows(c, 3);
    u32x4 hv[5];
#pragma unroll
    for (int s = 0; s < 4; ++s) {
      const u32x2 a = glu4(o.acc[0][s], o.acc[1][s], cg0, cu0, rs[s], mr[s]), b = glu4(o.acc[2][s], o.acc[3][s], cg1, cu1, rs[s], mr[s]);
      hv[s] = u32x4{a.x, a.y, b.x, b.y};
      if (s == 1) {
        STAMP(19);
        SCHED();
        wait_vm<8>();
        STAMP(20);
        w2_read<2>(c, w2r[2]); w2_read<3>(c, w2r[3]);
        wait_lgkm();
        SCHED();
        w2_issue<6, 2>(p, c); w2_issue<7, 3>(p, c);
        STAMP(21);
        SCHED();
      }
    }
    const f32x4 cgs = cs_rows(c, 4), cus = cs_rows(c, P1<TA>::NT - 1);
    hv[4] = pair_slots(glu4(o.acc[4][0], o.acc[P1<TA>::NT - 1][0], cgs, cus, rs[0], mr[0]), glu4(o.acc[4][1], o.acc[P1<TA>::NT - 1][1], cgs, cus, rs[1], mr[1]));
#pragma unroll
    for (int s = 0; s < 4; ++s) st_sc1(hv[s], rs_ws, a_off(tgp, c.q, c.n, s ^ cx, c.lane), 0);
    st_sc1(hv[4], rs_ws, a_off(tgp, (c.q & 6) + (c.n >> 1), 4, (rho & 1) ^ cx, c.i16 + 16 * (2 * (c.n & 1) + (rho >> 1))), 0);
    STAMP(3);
    SCHED();
    wait_bar<0>();
    if (c.w == 0 && c.lane == 0) flag_st(f2 + (8 + c.q) * 8, c.tag);
    STAMP(5);
    SCHED();
    w2_issue<8, 4>(p, c);
    SCHED();
  }

  relane(c);
  const char* f2c = p.ws + WS_F2 + (c.tg >> 1) * 256;
  const u32 abase = WS_A + (c.tg * 8 + c.w) * 9 * 4096 + c.lane * 16;
  u32x4 af[9][4];
  f32x4 acc[4][4];
#pragma unroll
  for (int v = 0; v < 4; ++v)
#pragma unroll
    for (int t = 0; t < 4; ++t) acc[v][t] = f32x4{0, 0, 0, 0};
  u32x4 kvl[8], zl[2];
  if (TA) {
    poll_tag(p.ws + WS_KVF + (c.q * 16 + c.i16) * 8, c.tag);
    STAMP(6);
    const u32 hd = c.q * 4 + c.n, u = c.lane & 31, ph = c.lane >> 5;
#pragma unroll
    for (int i = 0; i < 8; ++i) kvl[i] = ld_sc1(rs_ws, WS_KVP + ((hd * 16 + 8 * ph) * 2 + c.m) * 512 + u * 16, i * 1024);
#pragma unroll
    for (int s = 0; s < 2; ++s) zl[s] = ld_sc1(rs_ws, WS_ZP + (((hd * 2 + s) * 2 + ph) * 16 + c.i16) * 16, 0);
    SCHED();
  }
  {
    const u32 idx = (c.lane & 3) == 0 ? c.w : ((c.lane & 1) ? 8 + (c.w ^ 1) : 8 + c.w);
    poll_tag(f2c + idx * 8, c.tag);
  }
  STAMP(7);
#pragma unroll
  for (int s = 0; s < 5; ++s) p2_load(rs_ws, abase, s, af[s]);
  SCHED();
  if (TA) {
    float t[8];
#pragma unroll
    for (int j = 0; j < 8; ++j) t[j] = 0.f;
#pragma unroll
    for (int i = 0; i < 8; ++i)
#pragma unroll
      for (int j = 0; j < 4; ++j) { t[2 * j] += asf(kvl[i][j] << 16); t[2 * j + 1] += asf(kvl[i][j] & 0xffff0000u); }
    float r4[4];
#pragma unroll
    for (int j = 0; j < 4; ++j) { u32 a = asu(t[j]), b = asu(t[4 + j]); swap32(a, b); r4[j] = asf(a) + asf(b); }
    const u32 ex = L_SCR + (c.n * 2 + c.m) * 512;
    *(LAS u32x2*)(ex + (c.lane & 31) * 16 + (c.lane >> 5) * 8) = u32x2{pk2(r4[0], r4[1]), pk2(r4[2], r4[3])};
    SCHED();
    lgkm_bar();
    SCHED();
    u32x4 ka[2];
#pragma unroll
    for (int s = 0; s < 2; ++s) {
      const u32x4 kk = lds128(L_SCR + (c.n * 2 + s) * 512 + ((c.lane >> 5) * 16 + c.i16) * 16);
      const bool isz = c.lane & 16;
      ka[s] = u32x4{isz ? zl[s].x : kk.x, isz ? zl[s].y : kk.y, isz ? zl[s].z : kk.z, isz ? zl[s].w : kk.w};
    }
#pragma unroll
    for (int hf = 0; hf < 2; ++hf) {
      f32x16 y;
#pragma unroll
      for (int i = 0; i < 16; ++i) y[i] = 0.f;
      y = mfma32(ka[0], phq[hf][0], y);
      y = mfma32(ka[1], phq[hf][1], y);
      const float dn = ((y[8] + y[9]) + (y[10] + y[11])) + ((y[12] + y[13]) + (y[14] + y[15]));
      u32 a = asu(dn), b = a;
      swap32(a, b);
      const float inv = rcp(asf(a) + asf(b) + 1e-6f);
      const u32x4 ov = {pk2(y[0] * inv, y[1] * inv), pk2(y[2] * inv, y[3] * inv), pk2(y[4] * inv, y[5] * inv), pk2(y[6] * inv, y[7] * inv)};
      st_sc1(ov, rs_ws, a_off(tgp, c.q, 7 + (c.n >> 1), (2 * hf + (rho & 1)) ^ cx, c.i16 + 16 * (2 * (c.n & 1) + (rho >> 1))), 0);
    }
    STAMP(8);
    SCHED();
    p2_mma(acc, w2r[0], af[0]);
    p2_mma(acc, w2r[1], af[1]);
    SCHED();
    wait_bar<0>();
    if (c.w == 0 && c.lane == 0) flag_st(f2 + (16 + c.q) * 8, c.tag);
    STAMP(9);
    SCHED();
    w2_issue<8, 4>(p, c);
    p2_load(rs_ws, abase, 5, af[5]);
    p2_load(rs_ws, abase, 6, af[6]);
    SCHED();
  } else {
    p2_mma(acc, w2r[0], af[0]);
    SCHED();
    p2_load(rs_ws, abase, 5, af[5]);
    SCHED();
    p2_mma(acc, w2r[1], af[1]);
    SCHED();
    p2_load(rs_ws, abase, 6, af[6]);
    SCHED();
  }
  relane(c);
  const u32 tok = c.w * 8 + (c.lane >> 3), c8 = c.lane & 7;
  const size_t oo = (size_t)(c.tg * 64 + tok) * 1024 + c.cg * 128 + c8 * 16;
  const u32x4 xres = *(const GAS u32x4*)(p.x + oo);
  p2_mma(acc, w2r[2], af[2]);
  p2_mma(acc, w2r[3], af[3]);
  SCHED();
  poll_tag(f2c + (16 + c.w) * 8, c.tag);
  STAMP(10);
  p2_load(rs_ws, abase, 7, af[7]);
  p2_load(rs_ws, abase, 8, af[8]);
  SCHED();
  {
    u32x4 wv[4];
    w2_read<0>(c, wv); p2_mma(acc, wv, af[4]);
    w2_read<1>(c, wv); p2_mma(acc, wv, af[5]);
    w2_read<2>(c, wv); p2_mma(acc, wv, af[6]);
    w2_read<3>(c, wv); p2_mma(acc, wv, af[7]);
    w2_read<4>(c, wv); p2_mma(acc, wv, af[8]);
  }
  STAMP(11);
  SCHED();
  lgkm_bar();
  STAMP(23);
  SCHED();
#pragma unroll
  for (int v = 0; v < 4; ++v)
#pragma unroll
    for (int t = 0; t < 4; ++t)
      *(LAS f32x4*)(c.w * 16384 + (16 * t + c.i16) * 256 + (((4 * v + c.g) ^ (c.i16 & 7)) << 4)) = acc[v][t];
  SCHED();
  lgkm_bar();
  STAMP(24);
  SCHED();
  f32x4 r0 = {asf(xres.x << 16), asf(xres.x & 0xffff0000u), asf(xres.y << 16), asf(xres.y & 0xffff0000u)};
  f32x4 r1 = {asf(xres.z << 16), asf(xres.z & 0xffff0000u), asf(xres.w << 16), asf(xres.w & 0xffff0000u)};
#pragma unroll
  for (int ww = 0; ww < 8; ++ww) {
    r0 += lds128f(ww * 16384 + tok * 256 + (((2 * c8) ^ (tok & 7)) << 4));
    r1 += lds128f(ww * 16384 + tok * 256 + (((2 * c8 + 1) ^ (tok & 7)) << 4));
  }
  *(GAS u32x4*)(p.out + oo) = u32x4{pk2(r0[0], r0[1]), pk2(r0[2], r0[3]), pk2(r1[0], r1[1]), pk2(r1[2], r1[3])};
  STAMP(12);
  CSTAMP(31);
}

__global__ __launch_bounds__(512, 2) void fused_kernel(Params p) {
  __shared__ __attribute__((aligned(16))) char smem[163840];
  asm volatile("" ::"v"((LAS char*)smem));
  Ctx c;
  u32 stamps[2] = {0, 0};
  STAMP(0);
  c.lane = threadIdx.x & 63;
  c.w = __builtin_amdgcn_readfirstlane(threadIdx.x >> 6);
  c.m = c.w >> 2; c.n = c.w & 3; c.g = c.lane >> 4; c.i16 = c.lane & 15;
  const u32 b = blockIdx.x, xc = b & 7, r = b >> 3;
  c.ti = (xc & 1) * 8 + (r & 7);
  const u32 cjl = r >> 3;
  c.q = (xc >> 1) * 2 + (cjl & 1);
  c.tg = xc * 4 + (r & 3); c.cg = r >> 2;
  const u32 off = STAGGER ? ((2 * cjl + (r & 7)) & 7) : 0;
  {
    u64 pc; u32 len;
    asm volatile("s_getpc_b64 %0\n.Lfk_pc:\n\ts_mov_b32 %1, .Lfk_end-.Lfk_pc" : "=s"(pc), "=s"(len));
    u32 o = threadIdx.x * 128;
    o = o < len ? o : 0;
    asm volatile("s_mov_b32 m0, %2\n\ts_nop 0\n\tglobal_load_lds_dword %0, %1" ::"v"(o), "s"(pc), "s"(L_JUNK + c.w * 256) : "memory");
  }
  if (cjl < 2) {
    relane(c);
    if (c.w < 2) glds16s(p.wphi + c.q * 2048 + c.w * 1024, c.lane * 16, L_WPH + c.w * 1024);
    block_body<true>(p, c, off, stamps);
  } else {
    block_body<false>(p, c, off, stamps);
  }
  relane(c);
  if (b == 0 && c.w == 0) {
    const char* f = p.ws + WS_F2 + (c.lane >> 2) * 256 + (c.lane & 3) * 32;
    poll_tag(f, c.tag); poll_tag(f + 8, c.tag); poll_tag(f + 16, c.tag); poll_tag(f + 24, c.tag);
    if (c.lane == 0) flag_st(p.ws + WS_E, c.tag);
  }
  { const u32 d = (stamps[1] - stamps[0]) * DIAG_K; const u32 t0 = (u32)__builtin_amdgcn_s_memrealtime();
    while ((u32)__builtin_amdgcn_s_memrealtime() - t0 < d && d < 100000u) __builtin_amdgcn_s_sleep(2); }
  asm volatile(".Lfk_end:");
}

extern "C" void kernel_launch(void* const* d_in, const int* in_sizes, int n_in, void* d_out, int out_size, void* d_ws, size_t ws_size,
                              hipStream_t stream) {
  Params p;
  p.x = (const char*)d_in[0]; p.wqkv = (const char*)d_in[1]; p.wphi = (const char*)d_in[2]; p.wo = (const char*)d_in[3];
  p.wg = (const char*)d_in[5]; p.wu = (const char*)d_in[6]; p.wd = (const char*)d_in[7];
  p.out = (char*)d_out; p.ws = (char*)d_ws;
  fprintf(stderr, "ptrs x %p wqkv %p wphi %p wo %p wg %p wu %p wd %p out %p ws %p\n", p.x, p.wqkv, p.wphi, p.wo, p.wg, p.wu, p.wd, p.out, p.ws);
  fused_kernel<<<256, 512, 0, stream>>>(p);
}
```

```cpp
#include <hip/hip_runtime.h>
#include <stdint.h>
#pragma clang diagnostic ignored "-Wint-to-pointer-cast"

typedef unsigned u32;
typedef unsigned long long u64;
typedef __attribute__((ext_vector_type(4))) unsigned u32x4;
typedef __attribute__((ext_vector_type(2))) unsigned u32x2;
typedef __attribute__((ext_vector_type(2))) float f32x2;
typedef __attribute__((ext_vector_type(4))) float f32x4;
typedef __attribute__((ext_vector_type(16))) float f32x16;
typedef __attribute__((ext_vector_type(8))) __bf16 bf16x8;
typedef __attribute__((ext_vector_type(2))) __bf16 bf16x2;
typedef __attribute__((ext_vector_type(4))) short s16x4;
#define LAS __attribute__((address_space(3)))
#define GAS __attribute__((address_space(1)))
#define CAS __attribute__((address_space(4)))
#define DI __device__ __forceinline__

#define ABL_NOSWZ 0
#define ABL_NOCS 0
#define ABL_NOSTAT 0
#define ABL_NOLD 0
#define ABL_NORD 0
#define STATPOS 1
#define STAGGER 0
#define NOREFILL 0
#define BIGCH 0
#define W2PF 1
constexpr unsigned SPIN_CAP = 1u << 16;
#define ROT 1
#define CODE_PF 1

constexpr u32 WS_E = 0;
constexpr u32 WS_PF = 4096;
constexpr u32 WS_F2 = 2048;
constexpr u32 WS_STG = 8192;
constexpr u32 WS_CSG = 65536;
constexpr u32 WS_KVP = WS_CSG + 786432;
constexpr u32 WS_ZP = WS_KVP + 524288;
constexpr u32 WS_Q = WS_ZP + 32768;
constexpr u32 WS_A = 4u << 20;
constexpr u32 WS_BYTES = WS_A + 32u * 72u * 4096u;
constexpr u32 WS_DBG = 16u << 20;
static_assert(WS_Q + 2097152 <= WS_A && WS_BYTES <= WS_DBG, "ws map");

constexpr u32 L_X = 0;
constexpr u32 L_W = 49152;
constexpr u32 L_M = 147456;
constexpr u32 L_ST = L_M;
constexpr u32 L_CS = L_M + 1024;
constexpr u32 L_WPH = L_M + 4096;
constexpr u32 L_JUNK = L_M + 6144;
constexpr u32 L_SCR = L_X + 16384;

constexpr float LOG2E = 1.4426950408889634f;

struct Params {
  const char* x; const char* wqkv; const char* wphi; const char* wo;
  const char* wg; const char* wu; const char* wd;
  char* out; char* ws;
};

#define MEMFENCE() asm volatile("" ::: "memory")
#define SCHED() __builtin_amdgcn_sched_barrier(0)
template <int N> DI void wait_vm() { asm volatile("s_waitcnt vmcnt(%0)" ::"n"(N) : "memory"); }
template <int N> DI void wait_bar() { asm volatile("s_waitcnt vmcnt(%0) lgkmcnt(0)\n\ts_barrier" ::"n"(N) : "memory"); }
#define PRO_BAR() asm volatile("s_barrier" ::: "memory")
DI void lgkm0() { __builtin_amdgcn_s_waitcnt(0xC07F); }
DI void lgkm_bar() { asm volatile("s_waitcnt lgkmcnt(0)\n\ts_barrier" ::: "memory"); }
DI void wait_lgkm() { asm volatile("s_waitcnt lgkmcnt(0)" ::: "memory"); }

DI void glds16(const char* g, u32 lds) {
  asm volatile("s_mov_b32 m0, %1\n\ts_nop 0\n\tglobal_load_lds_dwordx4 %0, off" ::"v"(g), "s"(__builtin_amdgcn_readfirstlane(lds)) : "memory");
}
DI void glds16o128(const char* g, u32 lds) {
  asm volatile("s_mov_b32 m0, %1\n\ts_nop 0\n\tglobal_load_lds_dwordx4 %0, off offset:128" ::"v"(g), "s"(__builtin_amdgcn_readfirstlane(lds - 128)) : "memory");
}
DI void glds16s(const char* sbase, u32 voff, u32 lds) {
  asm volatile("s_mov_b32 m0, %2\n\ts_nop 0\n\tglobal_load_lds_dwordx4 %0, %1" ::"v"(voff), "s"(sbase), "s"(__builtin_amdgcn_readfirstlane(lds)) : "memory");
}
DI void junk_load(const char* g, u32 lds) {
  asm volatile("s_mov_b32 m0, %1\n\ts_nop 0\n\tglobal_load_lds_dword %0, off" ::"v"(g), "s"(__builtin_amdgcn_readfirstlane(lds)) : "memory");
}
DI u32x4 lds128(u32 a) { return *(const LAS u32x4*)(a); }
DI f32x4 lds128f(u32 a) { return *(const LAS f32x4*)(a); }
DI u32x2 ldstr(u32 a) { return __builtin_bit_cast(u32x2, __builtin_amdgcn_ds_read_tr16_b64_v4i16((LAS s16x4*)(a))); }
DI u32x4 ldstr2(u32 a0, u32 a1) { u32x2 lo = ldstr(a0), hi = ldstr(a1); return u32x4{lo.x, lo.y, hi.x, hi.y}; }
DI f32x4 mfma16(u32x4 a, u32x4 b, f32x4 c) {
  return __builtin_amdgcn_mfma_f32_16x16x32_bf16(__builtin_bit_cast(bf16x8, a), __builtin_bit_cast(bf16x8, b), c, 0, 0, 0);
}
DI f32x16 mfma32(u32x4 a, u32x4 b, f32x16 c) {
  return __builtin_amdgcn_mfma_f32_32x32x16_bf16(__builtin_bit_cast(bf16x8, a), __builtin_bit_cast(bf16x8, b), c, 0, 0, 0);
}
DI u32 pk2(float a, float b) { bf16x2 v = {(__bf16)a, (__bf16)b}; return __builtin_bit_cast(u32, v); }
DI float asf(u32 x) { return __builtin_bit_cast(float, x); }
DI u32 asu(float x) { return __builtin_bit_cast(u32, x); }
DI float fmax2(float a, float b) { return __builtin_elementwise_maximum(a, b); }
DI void swap16(u32& a, u32& b) { auto r = __builtin_amdgcn_permlane16_swap(a, b, false, false); a = r[0]; b = r[1]; }
DI void swap32(u32& a, u32& b) { auto r = __builtin_amdgcn_permlane32_swap(a, b, false, false); a = r[0]; b = r[1]; }
DI float ex2(float x) { return __builtin_amdgcn_exp2f(x); }
DI float rcp(float x) { return __builtin_amdgcn_rcpf(x); }

typedef __amdgpu_buffer_rsrc_t rsrc_t;
DI u32x4 ld_sc1(rsrc_t rs, u32 voff, u32 soff) { return __builtin_amdgcn_raw_buffer_load_b128(rs, voff, soff, 16); }
DI void st_sc1(u32x4 v, rsrc_t rs, u32 voff, u32 soff) { __builtin_amdgcn_raw_buffer_store_b128(v, rs, voff, soff, 16); }
DI u64 flag_ld(const char* p) { return __hip_atomic_load((GAS u64*)p, __ATOMIC_RELAXED, __HIP_MEMORY_SCOPE_AGENT); }
DI void flag_st(char* p, u64 v) { __hip_atomic_store((GAS u64*)p, v, __ATOMIC_RELAXED, __HIP_MEMORY_SCOPE_AGENT); }
DI void poll_tag(const char* p, u64 tag) {
#pragma unroll 1
  for (u32 spins = 0; spins < SPIN_CAP; ++spins) {
          MEMFENCE();
    u64 v = flag_ld(p);
    if (__all(v == tag)) break;
    __builtin_amdgcn_s_sleep(1);
  }
  __builtin_amdgcn_fence(__ATOMIC_ACQUIRE, "wavefront");
}

#define STAMP(i) do {} while (0)
#define CSTAMP(i) do {} while (0)
#define KSTAMP(i) do {} while (0)

template <int V> struct IC { static constexpr int value = V; constexpr operator int() const { return V; } };
template <int I, int N, class F> DI void static_for(F&& f) { if constexpr (I < N) { f(IC<I>{}); static_for<I + 1, N>(f); } }

struct Ctx {
  u32 rot;
  u32 ws;
  u32 w, m, n, lane, g, i16;
  u32 ti, q, cj, cg, tg;
  u64 tag;
};

DI void relane(Ctx& c) {
  u32 l = __builtin_amdgcn_mbcnt_hi(~0u, __builtin_amdgcn_mbcnt_lo(~0u, 0u));
  asm volatile("" : "+v"(l));
  c.lane = l; c.g = l >> 4; c.i16 = l & 15;
}
DI u32 step_id(const Ctx& c, int step) { return step < 4 ? (step + c.rot) & 3 : (step == 4 ? 4 : (step < 7 ? 5 + ((step - 5) ^ (c.rot & 1)) : 7 + ((step - 7) ^ (c.rot & 1)))); }
DI u32 w2_area_piece(u32 area, u32 piece, u32 w) {
  if (area < 4) return piece < 3 ? L_W + area * 24576 + w * 3072 + piece * 1024
                                 : L_X + (area < 2 ? 32768 : 0) + (area & 1) * 8192 + w * 1024;
  return piece < 2 ? L_SCR + w * 2048 + piece * 1024 : L_M + w * 2048 + (piece - 2) * 1024;
}
template <int STEP, int AREA> DI void w2_issue(const Params& p, const Ctx& c, int g0 = 0, int g1 = 4) {
  if (NOREFILL && STEP >= 4) return;
  const u32 j = c.lane >> 3;
  const u32 rowl = STEP < 4 ? 64 * (j >> 2) + (j & 3) : (STEP < 7 ? j : (j & 3) + 8 * (j >> 2));
  const u32 P0 = 8 + 5 * (c.ws >> 1) + 2 * (c.ws & 1), PS = 8 + 5 * (c.ws >> 1) + 4;
#pragma unroll
  for (int g = g0; g < g1; ++g) {
    const u32 sw = (((j >> 1) & 1) << 1) | ((g & 1) << 2);
    const u32 voff = rowl * 1024 + (((c.lane & 7) ^ sw) << 4);
    u32 rows;
    const u32 sid = step_id(c, STEP);
    if (STEP < 4) rows = 64 * P0 + 16 * sid + 4 * g;
    else if (STEP == 4) rows = 64 * PS + 32 * (c.ws & 1) + 8 * g;
    else if (STEP < 7) rows = 64 * c.ws + 32 * (sid - 5) + 8 * g;
    else rows = 64 * c.ws + 16 * (2 * (sid - 7) + (g >> 1)) + 4 * (g & 1);
    const char* base = (STEP < 7 ? p.wd : p.wo) + (size_t)(rows * 1024 + c.cg * 128);
    glds16s(base, voff, w2_area_piece(AREA, g, c.w));
  }
}
template <int AREA> DI void w2_read(const Ctx& c, u32x4 (&f)[4]) {
  const u32 qq = c.i16 >> 2, pp = c.i16 & 3;
  u32 pb;
  if (AREA < 4) pb = c.g < 3 ? L_W + AREA * 24576 + c.w * 3072 + c.g * 1024 : L_X + (AREA < 2 ? 32768 : 0) + (AREA & 1) * 8192 + c.w * 1024;
  else pb = c.g < 2 ? L_SCR + c.w * 2048 + c.g * 1024 : L_M + c.w * 2048 + (c.g - 2) * 1024;
  const u32 sw = (((qq >> 1) & 1) << 1) | ((c.g & 1) << 2);
  const u32 a = pb + qq * 128 + (((pp >> 1) ^ sw) << 4) + 8 * (pp & 1);
#pragma unroll
  for (int v = 0; v < 4; ++v) f[v] = ldstr2((a ^ (v * 32)), (a ^ (v * 32)) + 512);
}
DI u32 a_off(u32 tg, u32 wsl, u32 step, u32 tb, u32 lanep) { return WS_A + (((tg * 8 + wsl) * 9 + step) * 4 + tb) * 1024 + lanep * 16; }
DI u32 pair_off(const Ctx& c, u32 wsl, u32 step, u32 tb0) { return a_off(c.ti * 2 + c.m, wsl, step, tb0 + (c.g & 1), c.i16 + 16 * (2 * (c.n & 1) + (c.g >> 1))); }
DI u32 q_off(const Ctx& c) { return WS_Q + ((c.ti * 8 + c.q) * 8 + c.w) * 2048 + c.lane * 16; }
DI u32 kv_off(const Ctx& c) {
  const u32 d = c.lane & 31, h = c.lane >> 5, hd = c.q * 4 + c.n;
  const u32 sb = c.m ^ (c.ti & 1);
  return d < 16 ? WS_KVP + ((hd * 16 + c.ti) * 2 + sb) * 512 + (h * 16 + d) * 16 : WS_ZP + (((hd * 2 + sb) * 2 + h) * 16 + c.ti) * 16;
}
template <int N> DI float row_ror(float x) { return asf(__builtin_amdgcn_update_dpp(0u, asu(x), 0x120 + N, 0xf, 0xf, false)); }
DI float wave_sum(float x) {
  u32 a = asu(x), b = a;
  swap32(a, b); x = asf(a) + asf(b);
  a = asu(x); b = a;
  swap16(a, b); x = asf(a) + asf(b);
  x += row_ror<8>(x); x += row_ror<4>(x); x += row_ror<2>(x); x += row_ror<1>(x);
  return x;
}
DI u32 umax3(u32 a, u32 b, u32 c) { return __builtin_elementwise_max(__builtin_elementwise_max(a, b), c); }
DI u32 umax4(u32 m, u32x4 v) { return umax3(umax3(m, v.x, v.y), v.z, v.w); }

struct Pro {
  const char* xs; const char* wsrc[3];
  u32 xv[2], kap;
  u32x4 xrow;
};
DI u32 chunk_of(u32 kap, int c8) {
  return c8 == 0 ? (kap == 0 ? 1u : 0u) : (c8 == 1 ? kap : ((u32)(c8 - 1) < kap ? (u32)(c8 - 1) : (u32)c8));
}

template <bool TA> struct P1 {
  static constexpr int NT = TA ? 5 : 6;
  f32x4 acc[NT][4];
  f32x2 st[4];
  f32x4 cs[NT];
  u32x2 gp;
  u32x4 aw;
};

template <bool TA> DI void main_loop(const Params& p, Ctx& c, Pro& pr, rsrc_t rs_ws, P1<TA>& o, u32* stamps) {
  constexpr int NT = P1<TA>::NT;
  const u32 w = c.w, cx = TA ? 0u : (c.q & 1) * 2;
  auto issue_x = [&](int c8, int pc) __attribute__((always_inline)) {
    glds16s(pr.xs + chunk_of(pr.kap, c8) * 128, pr.xv[pc], L_X + (c8 % 3) * 16384 + (16 * w + 8 * pc) * 128);
  };
  auto issue_w1 = [&](int s, int pc) __attribute__((always_inline)) {
    const u32 k0 = chunk_of(pr.kap, s >> 1) * 64 + (s & 1) * 32;
    const u32 dst = L_W + (s & 3) * 24576 + w * 3072;
    if (TA) glds16(pr.wsrc[pc] + (size_t)(k0 * (pc == 1 ? 3584 : 3072)), dst + pc * 1024);
    else if (pc == 1) glds16o128(pr.wsrc[0] + (size_t)(k0 * 3584), dst + 1024);
    else glds16(pr.wsrc[pc] + (size_t)(k0 * 3584), dst + pc * 1024);
  };
  u32 xa[2][2];
  xa[0][0] = L_X + (64 * c.m + 16 * cx + c.i16) * 128 + ((c.g ^ (c.i16 >> 1)) << 4);
  xa[0][1] = xa[0][0] ^ 4096;
  xa[1][0] = xa[0][0] ^ 64; xa[1][1] = xa[0][1] ^ 64;
  u32 wa;
  {
    const u32 qq = c.i16 >> 2, pp = c.i16 & 3;
    const u32 sw = (((qq >> 1) & 1) << 1) | ((c.g & 1) << 2);
    wa = L_W + 2 * c.g * 3072 + qq * 128 + (((2 * c.n + (pp >> 1)) ^ sw) << 4) + 8 * (pp & 1);
  }
  u32 wb = wa + 49152;
  asm volatile("" : "+v"(wa), "+v"(wb));
  u32x4 xf[2][4], wf[2][NT];
  u32x2 wh[2][NT][2];
  auto read1 = [&](int s, int i) __attribute__((always_inline)) {
    const int b = s & 1;
    if (i >= 2 && i < 6) { xf[b][i - 2] = lds128(xa[s & 1][(i - 2) >> 1] + ((i - 2) & 1) * 2048 + ((s >> 1) % 3) * 16384); return; }
    const int k = i < 2 ? i : i - 4, l = k >> 1, t = k & 1;
    if (l >= NT) return;
    wh[b][l][t] = ldstr(((s & 2) ? wb : wa) + (s & 1) * 24576 + l * 512 + t * 3072);
    if (t) wf[b][l] = u32x4{wh[b][l][0].x, wh[b][l][0].y, wh[b][l][1].x, wh[b][l][1].y};
  };
  auto mm = [&](int b, int i) __attribute__((always_inline)) {
    if (i < 16) { o.acc[i >> 2][i & 3] = mfma16(wf[b][i >> 2], xf[b][i & 3], o.acc[i >> 2][i & 3]); return; }
    if (TA) o.acc[4][i & 3] = mfma16(xf[b][i & 3], wf[b][4], o.acc[4][i & 3]);
    else { const int l = 4 + ((i >> 1) & 1) * (NT - 5), t = i & 1; o.acc[l][t] = mfma16(wf[b][l], xf[b][t], o.acc[l][t]); }
  };
  auto issue1 = [&](int s, int pc) __attribute__((always_inline)) {
    if (s < 12) { if (pc == 0) { if (s <= 10) issue_x((s + 5) >> 1, (s + 5) & 1); } else issue_w1(s + 4, pc - 1); }
    else if (s == 12) w2_issue<0, 0>(p, c, pc, pc + 1);
    else if (s == 13) w2_issue<1, 1>(p, c, pc, pc + 1);
    else if (s == 14) w2_issue<2, 2>(p, c, pc, pc + 1);
    else w2_issue<3, 3>(p, c, pc, pc + 1);
  };
  auto cread = [&](int i) __attribute__((always_inline)) {
    if (i < 4) o.st[i] = *(const LAS f32x2*)(L_ST + (64 * c.m + 16 * (i ^ cx) + c.i16) * 8);
    else if (i < 4 + (TA ? 4 : 6)) o.cs[i - 4] = lds128f(L_CS + (c.n * 6 + i - 4) * 64 + c.g * 16);
    else if (TA && i == 8) o.cs[4][0] = *(const LAS float*)(L_CS + (c.n * 6 + 4) * 64 + c.i16 * 4);
    else if (i == 10) {
      const u32 a = L_WPH + c.n * 512 + (8 * (c.lane >> 5) + (c.i16 >> 2)) * 32 + (c.i16 & 3) * 8;
      o.aw = ldstr2(a, a + 128);
    }
  };
#pragma unroll
  for (int l = 0; l < NT; ++l)
#pragma unroll
    for (int t = 0; t < 4; ++t) o.acc[l][t] = f32x4{0, 0, 0, 0};
  f32x4 acs[NT];
  const u32x4 ones = {0x3F803F80u, 0x3F803F80u, 0x3F803F80u, 0x3F803F80u};
  u32x4 gq[8];
  u32 gv0;
  u32x4 gs;
  float csel[NT], rv[8], rs1, rs2;
  const u32 tag32 = (u32)(c.tag >> 32);
#pragma unroll
  for (int i = 0; i < 16; ++i) read1(0, i);
#define PIN(x) asm volatile("" : "+v"(x))
  auto side = [&](auto S_, auto I_) __attribute__((always_inline)) {
    constexpr int s = S_, i = I_;
    if constexpr (s == 5 && (i == 1 || i == 3)) {
#pragma unroll
      for (int d = (i >> 1) * 2; d < (i >> 1) * 2 + 2; ++d) { rv[2 * d] = asf(pr.xrow[d] << 16); rv[2 * d + 1] = asf(pr.xrow[d] & 0xffff0000u); PIN(rv[2 * d]); PIN(rv[2 * d + 1]); }
    }
    if constexpr (s == 5 && i == 5) { rs1 = (rv[0] + rv[1]) + (rv[2] + rv[3]); PIN(rs1); }
    if constexpr (s == 5 && i == 7) { rs1 += (rv[4] + rv[5]) + (rv[6] + rv[7]); PIN(rs1); }
    if constexpr ((s == 5 || s == 6) && i == 8) { float& x = s == 6 ? rs2 : rs1; u32 p0 = asu(x), p1 = p0; swap32(p0, p1); x = asf(p0) + asf(p1); PIN(x); }
    if constexpr ((s == 5 || s == 6) && i == 9) { float& x = s == 6 ? rs2 : rs1; u32 p0 = asu(x), p1 = p0; swap16(p0, p1); x = asf(p0) + asf(p1); PIN(x); }
    if constexpr ((s == 5 || s == 6) && i == 18) { float& x = s == 6 ? rs2 : rs1; x += row_ror<8>(x); x += row_ror<4>(x); PIN(x); }
    if constexpr ((s == 5 || s == 6) && i == 19) { float& x = s == 6 ? rs2 : rs1; x += row_ror<2>(x); x += row_ror<1>(x); PIN(x); if (s == 5) { rs1 *= 1.f / 512.f; rs2 = 0.f; PIN(rs1); } }
    if constexpr (s == 6 && (i & 1) && i < 8) {
#pragma unroll
      for (int d = i - 1; d < i + 1; ++d) { const float e = rv[d] - rs1; rs2 = __builtin_fmaf(e, e, rs2); }
      PIN(rs2);
    }
    if constexpr (s == 7 && i == 7) {
      const float rstd = __builtin_amdgcn_rsqf(rs2 * (1.f / 512.f) + 1e-5f);
      if (c.lane == 0) st_sc1(u32x4{asu(rstd), tag32, asu(rs1), tag32}, rs_ws, WS_STG + (c.ti * 128 + c.cj * 8 + w) * 16, 0);
    }
    if constexpr (s == 4 && (i == 7 || i == 8)) {
      const u32x4 sent = {~0u, ~0u, ~0u, ~0u};
      if (TA) { st_sc1(sent, rs_ws, q_off(c), (i - 7) * 1024); if (i == 8 && (c.lane & 31) <= 16) st_sc1(sent, rs_ws, kv_off(c), 0); }
      else st_sc1(sent, rs_ws, pair_off(c, c.q, 7 + (c.n >> 1), (i - 7) * 2), 0);
    }
    if constexpr (s == 7 && i == 9) if (w == 7 && c.lane == 0) flag_st(p.ws + WS_PF + (c.ti * 16 + c.cj) * 8, c.tag);
    if constexpr (s == 4 && i == 9) if (c.m == 0) {
      const u32 r = c.i16 & 3, t = c.i16 >> 2;
#pragma unroll
      for (int l = 0; l < NT; ++l) csel[l] = r == 0 ? acs[l][0] : (r == 1 ? acs[l][1] : (r == 2 ? acs[l][2] : acs[l][3]));
      const float v0 = t == 0 ? csel[0] : (t == 1 ? csel[1] : (t == 2 ? csel[2] : csel[3]));
      const u32 base = WS_CSG + ((((c.ti >> 3) * 16 + c.cj) * 8 + pr.kap) * 384 + c.n * 96) * 8;
      __builtin_amdgcn_raw_buffer_store_b64(u32x2{asu(v0), tag32}, rs_ws, base + (t * 16 + 4 * c.g + r) * 8, 0, 16);
      if (TA) { if (c.lane < 32) __builtin_amdgcn_raw_buffer_store_b64(u32x2{c.lane < 16 ? asu(acs[4][0]) : 0u, tag32}, rs_ws, base + (64 + c.lane) * 8, 0, 16); }
      else if (t < 2) __builtin_amdgcn_raw_buffer_store_b64(u32x2{asu(t == 0 ? csel[4] : csel[NT - 1]), tag32}, rs_ws, base + (64 + t * 16 + 4 * c.g + r) * 8, 0, 16);
    }
    if constexpr (s == 8 && i == 1) {
      gv0 = w >= 4 ? WS_CSG + ((c.ti >> 3) * 16 + c.cj) * 24576 + ((w - 4) * 64 + c.lane) * 16 : WS_STG + (c.ti * 128 + (w & 1) * 64 + c.lane) * 16;
      PIN(gv0);
    }
    if constexpr (s >= 8 && s <= 11 && (i == 8 || i == 9)) {
      constexpr int k = (s - 8) * 2 + (i - 8);
      asm volatile("s_bitcmp1_b32 %3, %4\n\ts_cbranch_scc0 1f\n\tglobal_load_dwordx4 %0, %1, %2 sc1\n1:"
                   : "=v"(gq[k]) : "v"(gv0), "s"(p.ws + k * 3072), "n"(0x70), "s"(w) : "memory", "scc");
    }
    if constexpr (s == 12 && i == 9)
      asm volatile("s_bitcmp1_b32 %3, %4\n\ts_cbranch_scc0 1f\n\tglobal_load_dwordx4 %0, %1, %2 sc1\n1:"
                   : "=v"(gs) : "v"(gv0), "s"(p.ws), "n"(0x03), "s"(w) : "memory", "scc");
    if constexpr (s == 14 && i == 8) {
      const u32 a = WS_PF + ((!TA && c.lane < 16) ? c.lane * 16 + c.q : (c.tg >> 1) * 16 + 8 + c.ws) * 8;
      asm volatile("global_load_dwordx2 %0, %1, %2 sc1" : "=v"(o.gp) : "v"(a), "s"(p.ws) : "memory");
    }
    if constexpr (s == 14 && i == 9) {
      asm volatile("" : "+v"(gq[0]), "+v"(gq[1]), "+v"(gq[2]), "+v"(gq[3]), "+v"(gq[4]), "+v"(gq[5]), "+v"(gq[6]), "+v"(gq[7]));
      if (w >= 4 && w < 7) {
        bool ok = true;
#pragma unroll
        for (int k = 0; k < 8; ++k) ok &= gq[k].y == tag32 && gq[k].w == tag32;
        if (!__all(ok)) {
#pragma unroll 1
          for (u32 spins = 0; spins < SPIN_CAP; ++spins) {
          MEMFENCE();
            ok = true;
#pragma unroll
            for (int k = 0; k < 8; ++k) { gq[k] = ld_sc1(rs_ws, gv0, k * 3072); ok &= gq[k].y == tag32 && gq[k].w == tag32; }
            if (__all(ok)) break;
          }
        }
        *(LAS f32x2*)(L_CS + ((w - 4) * 64 + c.lane) * 8) =
            f32x2{((asf(gq[0].x) + asf(gq[1].x)) + (asf(gq[2].x) + asf(gq[3].x))) + ((asf(gq[4].x) + asf(gq[5].x)) + (asf(gq[6].x) + asf(gq[7].x))),
                  ((asf(gq[0].z) + asf(gq[1].z)) + (asf(gq[2].z) + asf(gq[3].z))) + ((asf(gq[4].z) + asf(gq[5].z)) + (asf(gq[6].z) + asf(gq[7].z)))};
      }
    }
    if constexpr (s == 15 && i == 9) {
      asm volatile("" : "+v"(gs));
      if (w < 2) {
        if (!__all(gs.y == tag32 && gs.w == tag32)) {
#pragma unroll 1
          for (u32 spins = 0; spins < SPIN_CAP; ++spins) {
          MEMFENCE();
            gs = ld_sc1(rs_ws, gv0, 0);
            if (__all(gs.y == tag32 && gs.w == tag32)) break;
          }
        }
        *(LAS u32x2*)(L_ST + (w * 64 + c.lane) * 8) = u32x2{gs.x, gs.z};
      }
    }
  };
  SCHED();
  static_for<0, 16>([&](auto S_) __attribute__((always_inline)) {
    constexpr int s = S_;
    constexpr int b = s & 1;
    if (s == 4 || s == 7 || s == 8 || s == 10 || s == 13 || s == 14 || s == 15) relane(c);
    static_for<0, 10>([&](auto I_) __attribute__((always_inline)) {
      constexpr int i = I_;
      mm(b, i);
      if (s == 0 && i < 8) issue1(i < 4 ? -2 : -1, i & 3);
      if (s > 0 && (i & 1) == 0 && i < 8) issue1(s - 1, i >> 1);
      side(S_, I_);
      SCHED();
    });
    lgkm0();
    if (s == 15) wait_bar<63>();
    else if (s == 12 || s == 13) wait_bar<7>();
    else wait_bar<8>();
    if (s == 0) STAMP(27);
    if (s == 3) STAMP(28);
    if (s == 5) STAMP(15);
    if (s == 7) STAMP(16);
    if (s == 9) STAMP(17);
    if (s == 11) STAMP(21);
    if (s == 13) STAMP(25);
    if (s == 15) STAMP(26);
    if (s == 15) relane(c);
    SCHED();
    static_for<0, 10>([&](auto I_) __attribute__((always_inline)) {
      constexpr int i = I_;
      mm(b, 10 + i);
      if (s < 15 && i < 8) { read1(s + 1, 2 * i); read1(s + 1, 2 * i + 1); }
      if (s == 15) { if (i < 6) { cread(2 * i); cread(2 * i + 1); } if (i >= 6) issue1(15, i - 6); }
      side(S_, IC<10 + i>{});
      SCHED();
    });
    if constexpr (s == 2 || s == 3) if (c.m == 0) {
#pragma unroll
      for (int l = 0; l < NT; ++l) {
        const f32x4 z = {0, 0, 0, 0};
        if (TA && l == 4) acs[l] = mfma16(ones, wf[b][l], s == 2 ? z : acs[l]);
        else acs[l] = mfma16(wf[b][l], ones, s == 2 ? z : acs[l]);
      }
    }
    SCHED();
  });
}

DI f32x2 pfma(f32x2 a, f32x2 b, f32x2 c) { return __builtin_elementwise_fma(a, b, c); }
DI f32x2 lo2(f32x4 v) { return f32x2{v[0], v[1]}; }
DI f32x2 hi2(f32x4 v) { return f32x2{v[2], v[3]}; }
DI u32x2 glu4(f32x4 ag, f32x4 au, f32x4 cg, f32x4 cu, f32x2 st) {
  const float nrs = -LOG2E * st.x, irs = rcp(st.x), nm = -st.y;
  const f32x2 N = {nrs, nrs}, I = {irs, irs}, M = {nm, nm};
  const f32x2 a0 = pfma(M, lo2(cg), lo2(ag)) * N, a1 = pfma(M, hi2(cg), hi2(ag)) * N;
  const f32x2 d0 = pfma(f32x2{ex2(a0.x), ex2(a0.y)}, I, I), d1 = pfma(f32x2{ex2(a1.x), ex2(a1.y)}, I, I);
  const f32x2 h0 = pfma(M, lo2(cu), lo2(au)) * f32x2{rcp(d0.x), rcp(d0.y)}, h1 = pfma(M, hi2(cu), hi2(au)) * f32x2{rcp(d1.x), rcp(d1.y)};
  return u32x2{pk2(h0.x, h0.y), pk2(h1.x, h1.y)};
}
DI u32x2 ln4(f32x4 a, f32x4 cs, f32x2 st) {
  const f32x2 M = {-st.y, -st.y}, R = {st.x, st.x};
  const f32x2 y0 = pfma(M, lo2(cs), lo2(a)) * R, y1 = pfma(M, hi2(cs), hi2(a)) * R;
  return u32x2{pk2(y0.x, y0.y), pk2(y1.x, y1.y)};
}
DI u32x4 pair_slots(u32x2 x, u32x2 y) {
  u32 x0 = x.x, x1 = x.y, y0 = y.x, y1 = y.y;
  swap16(x0, y0); swap16(x1, y1);
  return u32x4{x0, x1, y0, y1};
}
template <class H> DI void features2(u32x4 aw, const u32x4 (&b)[2], u32x4 (&ph)[2][2], H&& hook) {
  f32x16 z, y[2];
#pragma unroll
  for (int i = 0; i < 16; ++i) z[i] = 0.f;
  y[0] = mfma32(aw, b[0], z); y[1] = mfma32(aw, b[1], z);
  SCHED(); hook(IC<0>{}); SCHED();
  u32 ma[2], mb[2];
#pragma unroll
  for (int f = 0; f < 2; ++f) {
    ma[f] = asu(fmax2(fmax2(fmax2(fmax2(y[f][0], y[f][1]), y[f][2]), fmax2(fmax2(y[f][3], y[f][4]), y[f][5])), fmax2(y[f][6], y[f][7])));
    mb[f] = asu(fmax2(fmax2(fmax2(fmax2(y[f][8], y[f][9]), y[f][10]), fmax2(fmax2(y[f][11], y[f][12]), y[f][13])), fmax2(y[f][14], y[f][15])));
  }
#pragma unroll
  for (int f = 0; f < 2; ++f) swap32(ma[f], mb[f]);
#pragma unroll
  for (int f = 0; f < 2; ++f) { ma[f] = asu(fmax2(asf(ma[f]), asf(mb[f])) * -LOG2E); mb[f] = ma[f]; }
#pragma unroll
  for (int f = 0; f < 2; ++f) swap32(ma[f], mb[f]);
  SCHED(); hook(IC<1>{}); SCHED();
  f32x2 e[2][8];
  static_for<0, 4>([&](auto K_) __attribute__((always_inline)) {
    constexpr int k = K_, f = k >> 1, o = (k & 1) * 4;
    const float m = asf(o ? mb[f] : ma[f]);
#pragma unroll
    for (int i = 0; i < 4; ++i) {
      const f32x2 a = pfma(f32x2{y[f][2 * (o + i)], y[f][2 * (o + i) + 1]}, f32x2{LOG2E, LOG2E}, f32x2{m, m});
      e[f][o + i] = f32x2{ex2(a.x), ex2(a.y)};
      asm volatile("" : "+v"(e[f][o + i]));
    }
    SCHED(); hook(IC<2 + k>{}); SCHED();
  });
#pragma unroll
  for (int f = 0; f < 2; ++f) {
    const f32x2 sa = (e[f][0] + e[f][1]) + (e[f][2] + e[f][3]), sb = (e[f][4] + e[f][5]) + (e[f][6] + e[f][7]);
    ma[f] = asu(sa.x + sa.y); mb[f] = asu(sb.x + sb.y);
  }
#pragma unroll
  for (int f = 0; f < 2; ++f) swap32(ma[f], mb[f]);
#pragma unroll
  for (int f = 0; f < 2; ++f) { ma[f] = asu(rcp(asf(ma[f]) + asf(mb[f]))); mb[f] = ma[f]; }
#pragma unroll
  for (int f = 0; f < 2; ++f) swap32(ma[f], mb[f]);
#pragma unroll
  for (int f = 0; f < 2; ++f)
#pragma unroll
    for (int i = 0; i < 4; ++i) {
      const f32x2 a = e[f][i] * f32x2{asf(ma[f]), asf(ma[f])}, b = e[f][4 + i] * f32x2{asf(mb[f]), asf(mb[f])};
      ph[f][0][i] = pk2(a.x, a.y); ph[f][1][i] = pk2(b.x, b.y);
    }
}

DI void p2_load(const Ctx& c, rsrc_t rs, u32 vbase, int step, u32x4 (&f)[4]) {
#pragma unroll
  for (int t = 0; t < 4; ++t) f[t] = ld_sc1(rs, vbase + t * 1024, step_id(c, step) * 4096);
}
DI void p2_mma(f32x4 (&acc)[4][4], const u32x4 (&wv)[4], const u32x4 (&af)[4]) {
#pragma unroll
  for (int t = 0; t < 4; ++t)
#pragma unroll
    for (int v = 0; v < 4; ++v) acc[v][t] = mfma16(wv[v], af[t], acc[v][t]);
}

template <bool TA> DI void block_body(const Params& p, Ctx& c, Pro& pr, rsrc_t rs_ws, u32* stamps) {
  P1<TA> o;
  main_loop<TA>(p, c, pr, rs_ws, o, stamps);
  STAMP(2);
  u32x4 w2r[4][4];
  char* f2 = p.ws + WS_F2 + c.ti * 128;
  const char* f2c = p.ws + WS_F2 + (c.tg >> 1) * 128;
  u32x4 af[9][4];
  f32x4 acc[4][4];
#pragma unroll
  for (int v = 0; v < 4; ++v)
#pragma unroll
    for (int t = 0; t < 4; ++t) acc[v][t] = f32x4{0, 0, 0, 0};
  const u32 sb = c.m ^ (c.ti & 1);
  {
    const u32 sg = (((c.lane >> 4) ^ sb) & 1) ? 0x80008000u : 0u;
    o.aw.x ^= sg; o.aw.y ^= sg; o.aw.z ^= sg; o.aw.w ^= sg;
  }
  const char* pfp = p.ws + WS_PF + ((!TA && c.lane < 16) ? c.lane * 16 + c.q : (c.tg >> 1) * 16 + 8 + c.ws) * 8;
  const char* fb = f2c + (8 + (c.ws ^ (c.lane & 1))) * 8;
  u64 pfl, pfa;
  SCHED();
  if (TA) {
#pragma unroll
    for (int f = 0; f < 2; ++f)
      st_sc1(pair_slots(ln4(o.acc[0][2 * f], o.cs[0], o.st[2 * f]), ln4(o.acc[0][2 * f + 1], o.cs[0], o.st[2 * f + 1])), rs_ws, q_off(c), f * 1024);
    SCHED();
    const u32 img = L_SCR + c.w * 2048;
    f32x16 kva, kvb;
    {
      u32x4 kb[2], ph[2][2], vb[4], pa[4];
      wait_vm<10>();
      asm volatile("" : "+v"(o.gp));
      if (!__all(o.gp.x == (u32)c.tag && o.gp.y == (u32)(c.tag >> 32))) poll_tag(pfp, c.tag);
      w2_read<0>(c, w2r[0]); w2_read<1>(c, w2r[1]);
#pragma unroll
      for (int f = 0; f < 2; ++f) kb[f] = pair_slots(ln4(o.acc[1][2 * f], o.cs[1], o.st[2 * f]), ln4(o.acc[1][2 * f + 1], o.cs[1], o.st[2 * f + 1]));
      features2(o.aw, kb, ph, [&](auto K_) __attribute__((always_inline)) {
        constexpr int k = K_;
        if (k == 0) { lgkm0(); MEMFENCE(); w2_issue<4, 0>(p, c, 0, 2); }
        if (k == 1) w2_issue<4, 0>(p, c, 2, 4);
        if (k == 3) w2_issue<5, 1>(p, c, 0, 2);
        if (k == 5) w2_issue<5, 1>(p, c, 2, 4);
      });
      f32x4 vst[4][2];
#pragma unroll
      for (int s = 0; s < 4; ++s) { const u32 sa = L_ST + (64 * c.m + 16 * s + 4 * c.g) * 8; vst[s][0] = lds128f(sa); vst[s][1] = lds128f(sa + 16); }
      const u32 t = c.lane & 31, h = c.lane >> 5, x = (t >> 1) & 7;
      const u32 qq = c.i16 >> 2, pp = c.i16 & 3, blk = (c.lane >> 4) & 1;
#pragma unroll
      for (int f = 0; f < 2; ++f) {
#pragma unroll
        for (int G = 0; G < 4; ++G)
          *(LAS u32x2*)(img + t * 64 + (((2 * G + h) ^ x) << 3)) = u32x2{ph[f][G >> 1][2 * (G & 1)], ph[f][G >> 1][2 * (G & 1) + 1]};
        MEMFENCE();
#pragma unroll
        for (int ks = 0; ks < 2; ++ks) {
          const u32 r0 = 16 * ks + 8 * h + qq, r1 = r0 + 4;
          pa[2 * f + ks] = ldstr2(img + r0 * 64 + (((4 * blk + pp) ^ ((r0 >> 1) & 7)) << 3), img + r1 * 64 + (((4 * blk + pp) ^ ((r1 >> 1) & 7)) << 3));
        }
        MEMFENCE();
      }
      const u32 cone = c.i16 == 0 ? 0x3F803F80u : 0u;
      const float csv = o.cs[4][0];
#pragma unroll
      for (int s = 0; s < 4; ++s) {
        const f32x4 s0 = vst[s][0], s1 = vst[s][1];
        const float v0 = __builtin_fmaf(-s0[1], csv, o.acc[4][s][0]) * s0[0], v1 = __builtin_fmaf(-s0[3], csv, o.acc[4][s][1]) * s0[2];
        const float v2 = __builtin_fmaf(-s1[1], csv, o.acc[4][s][2]) * s1[0], v3 = __builtin_fmaf(-s1[3], csv, o.acc[4][s][3]) * s1[2];
        vb[s] = pair_slots(u32x2{pk2(v0, v1), pk2(v2, v3)}, u32x2{cone, cone});
      }
      f32x16 z;
#pragma unroll
      for (int i = 0; i < 16; ++i) z[i] = 0.f;
      kva = mfma32(pa[0], vb[0], z); kvb = mfma32(pa[2], vb[2], z);
      kva = mfma32(pa[1], vb[1], kva); kvb = mfma32(pa[3], vb[3], kvb);
    }
    STAMP(22);
    {
      MEMFENCE();
      *(LAS f32x4*)(img + c.lane * 16) = f32x4{kva[8] + kvb[8], kva[9] + kvb[9], kva[10] + kvb[10], kva[11] + kvb[11]};
      *(LAS f32x4*)(img + 1024 + c.lane * 16) = f32x4{kva[12] + kvb[12], kva[13] + kvb[13], kva[14] + kvb[14], kva[15] + kvb[15]};
      SCHED();
      lgkm_bar();
      SCHED();
      const u32 pim = L_SCR + (c.w ^ 4) * 2048;
      const f32x4 k0 = lds128f(pim + c.lane * 16), k1 = lds128f(pim + 1024 + c.lane * 16);
      const u32x4 v = {pk2(kva[0] + kvb[0] + k0[0], kva[1] + kvb[1] + k0[1]), pk2(kva[2] + kvb[2] + k0[2], kva[3] + kvb[3] + k0[3]),
                       pk2(kva[4] + kvb[4] + k1[0], kva[5] + kvb[5] + k1[1]), pk2(kva[6] + kvb[6] + k1[2], kva[7] + kvb[7] + k1[3])};
      if ((c.lane & 31) <= 16) st_sc1(v, rs_ws, kv_off(c), 0);
    }
    STAMP(3);
    SCHED();
    wait_vm<11>();
    w2_read<2>(c, w2r[2]); w2_read<3>(c, w2r[3]);
    {
      u32x2 hp[4];
      static_for<0, 4>([&](auto S_) __attribute__((always_inline)) {
        constexpr int s = S_;
        hp[s] = glu4(o.acc[2][s], o.acc[3][s], o.cs[2], o.cs[3], o.st[s]);
        asm volatile("" : "+v"(hp[s]));
        SCHED();
        if (s == 0) { lgkm0(); MEMFENCE(); }
        if (s < 2) w2_issue<6, 2>(p, c, 2 * s, 2 * s + 2);
        if (s == 3) pfl = flag_ld(fb);
        SCHED();
      });
#pragma unroll
      for (int f = 0; f < 2; ++f) st_sc1(pair_slots(hp[2 * f], hp[2 * f + 1]), rs_ws, pair_off(c, c.q, 5 + (c.n >> 1), 2 * f), 0);
    }
    STAMP(4);
    SCHED();
    if (!__all(pfl == c.tag)) poll_tag(fb, c.tag);
    __builtin_amdgcn_fence(__ATOMIC_ACQUIRE, "wavefront");
    STAMP(7);
    const u32 abase = WS_A + (c.tg * 8 + c.ws) * 9 * 4096 + c.lane * 16;
    p2_load(c, rs_ws, abase, 0, af[0]);
    SCHED();
    w2_issue<7, 3>(p, c);
    SCHED();
    wait_bar<8>();
    if (c.w == 7 && c.lane == 0) flag_st(f2 + c.q * 8, c.tag);
    STAMP(5);
    SCHED();
    p2_load(c, rs_ws, abase, 1, af[1]);
    SCHED();
  } else {
    constexpr int NT = P1<TA>::NT;
    const u32 cx = (c.q & 1) * 2;
    KSTAMP(3);
    wait_vm<8>();
    KSTAMP(4);
    asm volatile("" : "+v"(o.gp));
    if (!__all(o.gp.x == (u32)c.tag && o.gp.y == (u32)(c.tag >> 32))) poll_tag(pfp, c.tag);
    w2_read<0>(c, w2r[0]); w2_read<1>(c, w2r[1]);
    KSTAMP(5);
    u32x2 hp[10];
    u32x4 qb[2];
    static_for<0, 10>([&](auto K_) __attribute__((always_inline)) {
      constexpr int k = K_, s = k < 8 ? (k >> 1) : k - 8, l = k < 8 ? 2 * (k & 1) : 4, u = k < 8 ? l + 1 : NT - 1;
      hp[k] = glu4(o.acc[l][s], o.acc[u][s], o.cs[l], o.cs[u], o.st[s]);
      if (BIGCH && !(k & 1)) return;
      asm volatile("" : "+v"(hp[k]), "+v"(hp[k & ~1]));
      SCHED();
      if (k == (BIGCH ? 1 : 0)) { lgkm0(); MEMFENCE(); }
      if (BIGCH && k == 1) w2_issue<4, 0>(p, c, 0, 2);
      if (k < 2) w2_issue<4, 0>(p, c, 2 * k, 2 * k + 2);
      if (k == 3) w2_issue<5, 1>(p, c, 0, 2);
      if (k == 5) w2_issue<5, 1>(p, c, 2, 4);
      if (k == 7) { qb[0] = ld_sc1(rs_ws, q_off(c), 0); qb[1] = ld_sc1(rs_ws, q_off(c), 1024); }
      KSTAMP(13 + k);
      SCHED();
    });
#pragma unroll
    for (int s = 0; s < 4; ++s) st_sc1(u32x4{hp[2 * s].x, hp[2 * s].y, hp[2 * s + 1].x, hp[2 * s + 1].y}, rs_ws, a_off(c.ti * 2 + c.m, c.q, c.n, s ^ cx, c.lane), 0);
    st_sc1(pair_slots(hp[8], hp[9]), rs_ws, pair_off(c, (c.q & 6) + (c.n >> 1), 4, cx), 0);
    KSTAMP(23);
    STAMP(3);
    SCHED();
    relane(c);
    if (!__all(umax4(umax4(0u, qb[0]), qb[1]) != ~0u)) {
#pragma unroll 1
      for (u32 spins = 0; spins < SPIN_CAP; ++spins) {
          MEMFENCE();
        qb[0] = ld_sc1(rs_ws, q_off(c), 0); qb[1] = ld_sc1(rs_ws, q_off(c), 1024);
        if (__all(umax4(umax4(0u, qb[0]), qb[1]) != ~0u)) break;
      }
    }
    MEMFENCE();
    w2_read<2>(c, w2r[2]); w2_read<3>(c, w2r[3]);
    STAMP(18);
    u32x4 phq[2][2];
    features2(o.aw, qb, phq, [&](auto K_) __attribute__((always_inline)) {
      constexpr int k = K_;
      if (k == 0) { lgkm0(); MEMFENCE(); }
      if (k < 2) w2_issue<6, 2>(p, c, 2 * k, 2 * k + 2);
      if (k == 2 || k == 3) w2_issue<7, 3>(p, c, 2 * k - 4, 2 * k - 2);
      if (k == 5) { wait_bar<8>(); if (c.w == 7 && c.lane == 0) flag_st(f2 + (8 + c.q) * 8, c.tag); STAMP(5); }
    });
#pragma unroll
    for (int f = 0; f < 2; ++f) asm volatile("" : "+v"(phq[f][0]), "+v"(phq[f][1]));
    STAMP(19);
    SCHED();
    u32x4 kvl[8], zl[2];
    const u32 kvo = WS_KVP + (((c.q * 4 + c.n) * 16 + 8 * (c.lane >> 5)) * 2 + sb) * 512 + (c.lane & 31) * 16;
    const u32 zo = WS_ZP + ((((c.q * 4 + c.n) * 2) * 2 + (c.lane >> 5)) * 16 + c.i16) * 16;
    pfl = flag_ld(fb);
#pragma unroll
    for (int i = 0; i < 8; ++i) kvl[i] = ld_sc1(rs_ws, kvo, i * 1024);
    zl[0] = ld_sc1(rs_ws, zo + sb * 512, 0); zl[1] = ld_sc1(rs_ws, zo + (sb ^ 1) * 512, 0);
    SCHED();
    if (!__all(pfl == c.tag)) poll_tag(fb, c.tag);
    __builtin_amdgcn_fence(__ATOMIC_ACQUIRE, "wavefront");
    STAMP(7);
    const u32 abase = WS_A + (c.tg * 8 + c.ws) * 9 * 4096 + c.lane * 16;
    p2_load(c, rs_ws, abase, 0, af[0]);
    SCHED();
    {
      u32 mx = umax4(umax4(0u, zl[0]), zl[1]);
#pragma unroll
      for (int i = 0; i < 8; ++i) mx = umax4(mx, kvl[i]);
      if (!__all(mx != ~0u)) {
#pragma unroll 1
        for (u32 spins = 0; spins < SPIN_CAP; ++spins) {
          MEMFENCE();
#pragma unroll
          for (int i = 0; i < 8; ++i) kvl[i] = ld_sc1(rs_ws, kvo, i * 1024);
          zl[0] = ld_sc1(rs_ws, zo + sb * 512, 0); zl[1] = ld_sc1(rs_ws, zo + (sb ^ 1) * 512, 0);
          mx = umax4(umax4(0u, zl[0]), zl[1]);
#pragma unroll
          for (int i = 0; i < 8; ++i) mx = umax4(mx, kvl[i]);
          if (__all(mx != ~0u)) break;
        }
      }
    }
    STAMP(6);
    SCHED();
    p2_load(c, rs_ws, abase, 1, af[1]);
    SCHED();
    float t[8];
#pragma unroll
    for (int j = 0; j < 8; ++j) t[j] = 0.f;
#pragma unroll
    for (int i = 0; i < 8; ++i)
#pragma unroll
      for (int j = 0; j < 4; ++j) { t[2 * j] += asf(kvl[i][j] << 16); t[2 * j + 1] += asf(kvl[i][j] & 0xffff0000u); }
    float r4[4];
#pragma unroll
    for (int j = 0; j < 4; ++j) { u32 a = asu(t[j]), b = asu(t[4 + j]); swap32(a, b); r4[j] = asf(a) + asf(b); }
    *(LAS u32x2*)(L_SCR + (c.n * 2 + sb) * 512 + (c.lane & 31) * 16 + (c.lane >> 5) * 8) = u32x2{pk2(r4[0], r4[1]), pk2(r4[2], r4[3])};
    SCHED();
    lgkm_bar();
    STAMP(4);
    SCHED();
    u32x4 ka[2];
#pragma unroll
    for (int s = 0; s < 2; ++s) {
      const u32x4 kk = lds128(L_SCR + (c.n * 2 + (s ^ sb)) * 512 + ((c.lane >> 5) * 16 + c.i16) * 16);
      const bool isz = c.lane & 16;
      ka[s] = u32x4{isz ? zl[s].x : kk.x, isz ? zl[s].y : kk.y, isz ? zl[s].z : kk.z, isz ? zl[s].w : kk.w};
    }
    u32x4 ov[2];
#pragma unroll
    for (int f = 0; f < 2; ++f) {
      f32x16 y;
#pragma unroll
      for (int i = 0; i < 16; ++i) y[i] = 0.f;
      y = mfma32(ka[0], phq[f][0], y);
      y = mfma32(ka[1], phq[f][1], y);
      const float dn = ((y[8] + y[9]) + (y[10] + y[11])) + ((y[12] + y[13]) + (y[14] + y[15]));
      u32 a = asu(dn), b = a;
      swap32(a, b);
      const float inv = rcp(asf(a) + asf(b) + 1e-6f);
      ov[f] = u32x4{pk2(y[0] * inv, y[1] * inv), pk2(y[2] * inv, y[3] * inv), pk2(y[4] * inv, y[5] * inv), pk2(y[6] * inv, y[7] * inv)};
    }
    SCHED();
#pragma unroll
    for (int f = 0; f < 2; ++f) st_sc1(ov[f], rs_ws, pair_off(c, c.q, 7 + (c.n >> 1), 2 * f), 0);
    STAMP(8);
    SCHED();
    lgkm_bar();
    SCHED();
  }

  relane(c);
  const u32 abase = WS_A + (c.tg * 8 + c.ws) * 9 * 4096 + c.lane * 16;
  const u32 ot = c.w >> 1, ov = 2 * (c.w & 1) + (c.lane >> 5), oh = (c.lane >> 4) & 1;
  const size_t oo = (size_t)(c.tg * 64 + 16 * ot + c.i16) * 1024 + c.cg * 128 + ov * 32 + oh * 16;
  p2_load(c, rs_ws, abase, 2, af[2]);
  SCHED();
  p2_mma(acc, w2r[0], af[0]);
  SCHED();
  pfa = flag_ld(f2c + c.ws * 8);
  p2_load(c, rs_ws, abase, 3, af[3]);
  w2_read<0>(c, w2r[0]);
  SCHED();
  p2_mma(acc, w2r[1], af[1]);
  SCHED();
  p2_load(c, rs_ws, abase, 4, af[4]);
  w2_read<1>(c, w2r[1]);
  SCHED();
  p2_mma(acc, w2r[2], af[2]);
  SCHED();
  if (!__all(pfa == c.tag)) poll_tag(f2c + c.ws * 8, c.tag);
  __builtin_amdgcn_fence(__ATOMIC_ACQUIRE, "wavefront");
  STAMP(9);
  p2_load(c, rs_ws, abase, 5, af[5]);
  w2_read<2>(c, w2r[2]);
  SCHED();
  p2_mma(acc, w2r[3], af[3]);
  SCHED();
  w2_issue<8, 4>(p, c);
  p2_load(c, rs_ws, abase, 6, af[6]);
  w2_read<3>(c, w2r[3]);
  SCHED();
  p2_mma(acc, w2r[0], af[4]);
  SCHED();
  p2_load(c, rs_ws, abase, 7, af[7]);
  SCHED();
  p2_mma(acc, w2r[1], af[5]);
  SCHED();
  p2_load(c, rs_ws, abase, 8, af[8]);
  const u32x4 xres = *(const GAS u32x4*)(p.x + oo);
  u32x4 ef[2];
  const bool bump = c.tg == 0 && c.cg == 0 && c.w == 0;
  if (bump) { ef[0] = ld_sc1(rs_ws, WS_F2 + c.lane * 32, 0); ef[1] = ld_sc1(rs_ws, WS_F2 + c.lane * 32 + 16, 0); }
  SCHED();
  p2_mma(acc, w2r[2], af[6]);
  SCHED();
  w2_read<4>(c, w2r[0]);
  STAMP(10);
  SCHED();
  {
    u32 mx = 0;
#pragma unroll
    for (int t = 0; t < 4; ++t) mx = umax4(umax4(mx, af[7][t]), af[8][t]);
    if (!__all(mx != ~0u)) {
#pragma unroll 1
      for (u32 spins = 0; spins < SPIN_CAP; ++spins) {
          MEMFENCE();
        p2_load(c, rs_ws, abase, 7, af[7]);
        p2_load(c, rs_ws, abase, 8, af[8]);
        mx = 0;
#pragma unroll
        for (int t = 0; t < 4; ++t) mx = umax4(umax4(mx, af[7][t]), af[8][t]);
        if (__all(mx != ~0u)) break;
      }
    }
  }
  STAMP(20);
  p2_mma(acc, w2r[3], af[7]);
  p2_mma(acc, w2r[0], af[8]);
  STAMP(11);
  SCHED();
#pragma unroll
  for (int v = 0; v < 4; ++v)
#pragma unroll
    for (int t = 0; t < 4; ++t) *(LAS f32x4*)(w2_area_piece(v, t, c.w) + c.lane * 16) = acc[v][t];
  STAMP(23);
  SCHED();
  lgkm_bar();
  STAMP(24);
  SCHED();
  f32x4 r0 = {asf(xres.x << 16), asf(xres.x & 0xffff0000u), asf(xres.y << 16), asf(xres.y & 0xffff0000u)};
  f32x4 r1 = {asf(xres.z << 16), asf(xres.z & 0xffff0000u), asf(xres.w << 16), asf(xres.w & 0xffff0000u)};
  {
    const u32 rb = (ot < 3 ? L_W + ov * 24576 + ot * 1024 : L_X + (ov < 2 ? 32768 : 0) + (ov & 1) * 8192) + (c.i16 + 32 * oh) * 16;
    const u32 st = ot < 3 ? 3072 : 1024;
#pragma unroll
    for (int ww = 0; ww < 8; ++ww) { r0 += lds128f(rb + ww * st); r1 += lds128f(rb + ww * st + 256); }
  }
  *(GAS u32x4*)(p.out + oo) = u32x4{pk2(r0[0], r0[1]), pk2(r0[2], r0[3]), pk2(r1[0], r1[1]), pk2(r1[2], r1[3])};
  if (bump) {
    const u32 tl = (u32)c.tag, th = (u32)(c.tag >> 32);
    const bool ok = ef[0].x == tl && ef[0].y == th && ef[0].z == tl && ef[0].w == th && ef[1].x == tl && ef[1].y == th && ef[1].z == tl && ef[1].w == th;
    if (!__all(ok)) {
      const char* f = p.ws + WS_F2 + c.lane * 32;
      poll_tag(f, c.tag); poll_tag(f + 8, c.tag); poll_tag(f + 16, c.tag); poll_tag(f + 24, c.tag);
    }
    if (c.lane == 0) flag_st(p.ws + WS_E, c.tag);
  }
  STAMP(12);
  CSTAMP(31);
}

__global__ __launch_bounds__(512, 2) void fused_kernel(Params p) {
  __shared__ __attribute__((aligned(16))) char smem[163840];
  asm volatile("" ::"v"((LAS char*)smem));
  Ctx c;
  u32* stamps = nullptr;
  STAMP(0);
  c.w = __builtin_amdgcn_readfirstlane(threadIdx.x >> 6);
  {
    u64 pc; u32 len;
    asm volatile("s_getpc_b64 %0\n.Lfk_pc:\n\ts_mov_b32 %1, .Lfk_end-.Lfk_pc\n\ts_max_u32 %1, %1, .Lfk_end2-.Lfk_pc" : "=s"(pc), "=s"(len) : : "scc");
    u32 o = threadIdx.x * 128;
    o = o < len ? o : 0;
    asm volatile("s_mov_b32 m0, %2\n\ts_nop 0\n\tglobal_load_lds_dword %0, %1" ::"v"(o), "s"(pc), "s"(L_JUNK + c.w * 256) : "memory");
  }
  SCHED();
  const u32 lane = threadIdx.x & 63, w = c.w;
  u32 b = blockIdx.x;
  Pro pr;
  u64 ep0;
  pr.kap = (b >> 3) & 7;
  c.ti = (b & 1) * 8 + pr.kap;
  pr.xs = p.x + (size_t)c.ti * 131072;
  const u32 kc0 = chunk_of(pr.kap, 0);
#pragma unroll
  for (int pc = 0; pc < 2; ++pc) pr.xv[pc] = (16 * w + 8 * pc + (lane >> 3)) * 1024 + (((lane & 7) ^ (4 * pc + (lane >> 4))) << 4);
  STAMP(13);
  glds16s(pr.xs + kc0 * 128, pr.xv[0], L_X + (16 * w) * 128);
  glds16s(pr.xs + kc0 * 128, pr.xv[1], L_X + (16 * w + 8) * 128);
  asm volatile("" : "+s"(b));
  SCHED();
  c.lane = lane; c.m = w >> 2; c.n = w & 3; c.g = lane >> 4; c.i16 = lane & 15;
  const u32 xc = b & 7, r = b >> 3;
  const bool ta = r < 16;
  c.q = (xc >> 1) * 2 + ((r >> 3) & 1);
  c.cj = (ta ? 0 : 8) + c.q;
  c.tg = xc * 4 + (r & 3); c.cg = r >> 2;
  c.rot = ROT ? (c.cg + r) & 3 : 0;
  c.ws = ROT ? (w + c.cg + 2 * (r & 3)) & 7 : w;
  {
    const u32 rr = (lane >> 3) & 3, hi = lane >> 5;
    const u32 sw = (((rr >> 1) & 1) << 1) | (((w >> 1) & 1) << 2);
    const u32 ch = (((lane & 7) ^ sw) << 4), kr = 4 * w + rr;
    const u32 P0 = 8 + 5 * (c.q >> 1) + 2 * (c.q & 1), PS = 8 + 5 * (c.q >> 1) + 4;
    const u32 pit = ta ? 3072 : 3584;
    const char* lo0 = ta ? p.wqkv + c.q * 128 : p.wg + P0 * 128;
    const char* hi0 = ta ? p.wqkv + 1024 + c.q * 128 : p.wu + P0 * 128;
    const char* lo2 = ta ? p.wqkv + 2048 + c.q * 128 : p.wg + PS * 128;
    const char* hi2 = ta ? lo2 : p.wu + PS * 128;
    const u32 c1 = (ta ? c.q : P0 + 1) * 128;
    pr.wsrc[0] = (hi ? hi0 : lo0) + (kr * pit + ch);
    pr.wsrc[1] = (hi ? p.wu : p.wg) + (kr * 3584 + c1 + ch);
    pr.wsrc[2] = (hi ? hi2 : lo2) + (kr * pit + ch);
#pragma unroll
    for (int s = 0; s < 2; ++s) {
      const u32 k0 = kc0 * 64 + s * 32, dst = L_W + s * 24576 + w * 3072;
      if (s == 1) {
        PRO_BAR();
        asm volatile("s_load_dwordx2 %0, %1, 0x0" : "=s"(ep0) : "s"(p.ws + WS_E) : "memory");
        asm volatile("global_load_dwordx4 %0, %1, %2" : "=v"(pr.xrow) : "v"((c.cj * 8 + w) * 1024 + lane * 16), "s"(pr.xs) : "memory");
        if (w < 2) glds16s(p.wphi + c.q * 2048 + w * 1024, lane * 16, L_WPH + w * 1024);
        glds16s(pr.xs + pr.kap * 128, pr.xv[0], L_X + 16384 + (16 * w) * 128);
      }
      glds16(pr.wsrc[0] + (size_t)(k0 * pit), dst);
      glds16(pr.wsrc[1] + (size_t)(k0 * 3584), dst + 1024);
      glds16(pr.wsrc[2] + (size_t)(k0 * pit), dst + 2048);
    }
  }
  SCHED();
  const rsrc_t rs_ws = __builtin_amdgcn_make_buffer_rsrc(p.ws, 0, WS_BYTES, 0x00020000);
  asm volatile("s_waitcnt vmcnt(4) lgkmcnt(0)\n\ts_barrier" : "+s"(ep0), "+v"(pr.xrow) : : "memory");
  {
    u64 ep = ep0 + 0x9E3779B97F4A7C15ull;
    ep = (ep ^ (ep >> 30)) * 0xBF58476D1CE4E5B9ull;
    ep = (ep ^ (ep >> 27)) * 0x94D049BB133111EBull;
    c.tag = ep ^ (ep >> 31);
  }
  STAMP(1);
  SCHED();
  if (ta) {
    block_body<true>(p, c, pr, rs_ws, stamps);
    asm volatile(".Lfk_end2:");
    __builtin_amdgcn_endpgm();
  }
  {
    block_body<false>(p, c, pr, rs_ws, stamps);
  }
  asm volatile(".Lfk_end:");
}

extern "C" void kernel_launch(void* const* d_in, const int* in_sizes, int n_in, void* d_out, int out_size, void* d_ws, size_t ws_size,
                              hipStream_t stream) {
  Params p;
  p.x = (const char*)d_in[0]; p.wqkv = (const char*)d_in[1]; p.wphi = (const char*)d_in[2]; p.wo = (const char*)d_in[3];
  p.wg = (const char*)d_in[5]; p.wu = (const char*)d_in[6]; p.wd = (const char*)d_in[7];
  p.out = (char*)d_out; p.ws = (char*)d_ws;
  fused_kernel<<<256, 512, 0, stream>>>(p);
}
```
